```python
import torch

def _softmax(x, axis=-1):
    return torch.softmax(x, dim=axis)
def _gelu(x):
    return torch.nn.functional.gelu(x, approximate="tanh")
def _silu(x):
    return torch.nn.functional.silu(x)
def _sigmoid(x):
    return torch.sigmoid(x)
def _layernorm(x, axis=-1, eps=1e-5):
    m = x.mean(dim=axis, keepdim=True)
    v = x.var(dim=axis, keepdim=True, unbiased=False)
    return (x - m) / torch.sqrt(v + eps)
def _scatter_add(tgt, idx, src, axis=0):
    return tgt.clone().index_add_(axis, idx, src)
def _segment_sum(src, seg, n):
    out = torch.zeros((n,) + src.shape[1:], dtype=src.dtype)
    return out.index_add_(0, seg, src)
def _pad(x, pw):
    flat = tuple(v for pair in reversed(pw) for v in pair)
    return torch.nn.functional.pad(x, flat)

kernel_name = 'concat_heads__mha_block__grouped_kv_mqa__prompt'

def setup_inputs():

    _w = (lambda a: (lambda t: t.to(torch.bfloat16) if t.is_floating_point() else t)(torch.as_tensor(a)))
    def _A_setup():
        B, S, D, H = 1, 2048, 640, 32
        return {
            "x": _w((lambda *s: torch.randn(*s, dtype=torch.bfloat16) * 0.2)(B, S, D)),
            "wqkv": _w((lambda *s: torch.randn(*s, dtype=torch.bfloat16) * 0.2)(D, 3 * D)),
            "wo": _w((lambda *s: torch.randn(*s, dtype=torch.bfloat16) * 0.05)(D, D)),
        }
    _A_in = _A_setup()

    def _B_setup():
        B, S, D, H = 1, 2048, 640, 32
        Dh = D // H
        return {
            "x": _w((lambda *s: torch.randn(*s, dtype=torch.bfloat16) * 0.05)(B, S, D)),
            "wq": _w((lambda *s: torch.randn(*s, dtype=torch.bfloat16) * 0.8)(D, H * Dh)),
            "wk": _w((lambda *s: torch.randn(*s, dtype=torch.bfloat16) * 0.8)(D, Dh)),
            "wv": _w((lambda *s: torch.randn(*s, dtype=torch.bfloat16) * 0.1)(D, Dh)),
            "wo": _w((lambda *s: torch.randn(*s, dtype=torch.bfloat16) * 0.1)(H * Dh, D)),
        }
    _B_in = _B_setup()

    return {
        "A_x": _A_in["x"],
        "A_wqkv": _A_in["wqkv"],
        "A_wo": _A_in["wo"],
        "B_x": _B_in["x"],
        "B_wq": _B_in["wq"],
        "B_wk": _B_in["wk"],
        "B_wv": _B_in["wv"],
        "B_wo": _B_in["wo"],
    }

def reference(A_x, A_wqkv, A_wo, B_x, B_wq, B_wk, B_wv, B_wo):
    def _A_ref(x, wqkv, wo):
        B, S, D = x.shape
        H = 32
        Dh = D // H
        qkv = (lambda x, *d: x.permute(*d))((x @ wqkv).reshape(B, S, 3, H, Dh), 2, 0, 3, 1, 4)
        q, k, v = qkv[0], qkv[1], qkv[2]
        scores = torch.einsum("bhqd,bhkd->bhqk", q, k) * (Dh ** -0.5)
        p = _softmax(scores, axis=-1)
        o = torch.einsum("bhqk,bhkd->bhqd", p, v)
        return (lambda x, *d: x.permute(*d))(o, 0, 2, 1, 3).reshape(B, S, D) @ wo

    def _B_ref(x, wq, wk, wv, wo):
        B, S, D = x.shape
        H = 32
        Dh = D // H
        q = (lambda x, *d: x.permute(*d))((x @ wq).reshape(B, S, H, Dh), 0, 2, 1, 3)
        k = x @ wk
        v = x @ wv
        scores = torch.einsum("bhqd,bkd->bhqk", q, k) * (Dh ** -0.5)
        p = _softmax(scores, axis=-1)
        o = torch.einsum("bhqk,bkd->bhqd", p, v)
        return (lambda x, *d: x.permute(*d))(o, 0, 2, 1, 3).reshape(B, S, H * Dh) @ wo

    a = _A_ref(A_x, A_wqkv, A_wo)
    b = _B_ref(B_x, B_wq, B_wk, B_wv, B_wo)
    return torch.concatenate([a, b], axis=-1)
```

```cpp
#include <hip/hip_runtime.h>
#include <hip/hip_bf16.h>
#include <stdint.h>

typedef float f32x16 __attribute__((ext_vector_type(16)));
typedef float f32x4 __attribute__((ext_vector_type(4)));
typedef __bf16 bf16x8 __attribute__((ext_vector_type(8)));
typedef __bf16 bf16x2 __attribute__((ext_vector_type(2)));
typedef float f32x2 __attribute__((ext_vector_type(2)));
typedef short s16x4 __attribute__((ext_vector_type(4)));
typedef short s16x8 __attribute__((ext_vector_type(8)));
typedef unsigned u32x4 __attribute__((ext_vector_type(4)));
typedef unsigned u32x2 __attribute__((ext_vector_type(2)));
typedef __attribute__((address_space(3))) const char* lds_cptr;
typedef __attribute__((address_space(3))) char* lds_ptr;
typedef __attribute__((address_space(3))) volatile unsigned* lds_vu;
typedef short v4i16_t __attribute__((ext_vector_type(4)));
typedef __amdgpu_buffer_rsrc_t rsrc_t;
extern "C" __device__ unsigned long long amdgcn_dispatch_id() __asm("llvm.amdgcn.dispatch.id");

constexpr int REC = 2944, NREC = 65, STREAM = REC * NREC, SLOT = 8 * REC;
constexpr int OFF_K2 = 1024, OFF_VS1 = 1536, OFF_VS0 = 2240;
constexpr unsigned Q1_OFF = 0, Q2_OFF = 4u << 20, KV_OFF = 5u << 20, O_OFF = 12u << 20, FL_OFF = 17u << 20, WS_USED = 18u << 20;
constexpr int F_O = 0, F_PA = 2048, F_PB = 3072;
constexpr int LDS_BYTES = 163840, WOIMG = 49152, BADW = 151552, POLLW = 40960;
constexpr float QSCALE = 0.22360679774997896f * 1.4426950408889634f;
constexpr unsigned SPIN_MAX = 1u << 18;
constexpr unsigned CODE_BYTES = 59392;
#define FUSED_SYM "_Z12fused_kernelPKcS0_S0_S0_S0_S0_S0_S0_PcS1_"
constexpr unsigned TLBPF_STRIDE = 65536;

#define ATTN_ASM \
  "s_mov_b32 %[sv], m0\n" \
  "s_mov_b64 s[60:61], %[dma]\n" \
  "v_mov_b64 v[192:193], %[k1a]\n" \
  "v_mov_b64 v[194:195], %[k1b]\n" \
  "v_mov_b64 v[196:197], %[k2]\n" \
  "v_mov_b64 v[198:199], %[v0a]\n" \
  "v_mov_b64 v[200:201], %[v0b]\n" \
  "v_mov_b32 v202, 0\n" \
  "v_mov_b32 v203, 0\n" \
  "v_mov_b32 v204, 0\n" \
  "v_mov_b32 v205, 0\n" \
  "v_mov_b32 v184, 0\n" \
  "v_mov_b32 v185, 0\n" \
  "v_mov_b32 v186, 0\n" \
  "v_mov_b32 v187, 0\n" \
  "v_mov_b32 v188, 0\n" \
  "v_mov_b32 v189, 0\n" \
  "v_mov_b32 v190, 0\n" \
  "v_mov_b32 v191, 0\n" \
  "s_nop 1\n" \
  "v_mfma_f32_32x32x16_bf16 v[128:143], v[192:195], %[q0], 0\n" \
  "v_mfma_f32_32x32x16_bf16 v[144:159], v[192:195], %[q1], 0\n" \
  "v_mfma_f32_32x32x8_bf16 v[128:143], v[196:197], %[r0], v[128:143]\n" \
  "v_mfma_f32_32x32x8_bf16 v[144:159], v[196:197], %[r1], v[144:159]\n" \
  "s_waitcnt vmcnt(0)\n" \
  "s_barrier\n" \
  "s_mov_b32 m0, %[m0b]\n" \
  "s_nop 0\n" \
  "global_load_lds_dwordx4 %[d0], s[60:61] sc1\n" \
  "global_load_lds_dwordx4 %[d1], s[60:61] offset:1024 sc1\n" \
  "global_load_lds_dwordx4 %[d2], s[60:61] offset:2048 sc1\n" \
  "s_add_u32 s60, s60, 11776\n" \
  "s_addc_u32 s61, s61, 0\n" \
  "s_mov_b64 s[62:63], %[wsrc]\n" \
  "s_mov_b32 s64, %[wdst]\n" \
  "s_nop 7\n" \
  "s_branch .Lentry%=\n" \
  ".Lloop%=:\n" \
  "s_waitcnt vmcnt(0) lgkmcnt(0)\n" \
  "s_barrier\n" \
  "s_mov_b32 m0, %[m0b]\n" \
  "s_nop 0\n" \
  "global_load_lds_dwordx4 %[d0], s[60:61] sc1\n" \
  "global_load_lds_dwordx4 %[d1], s[60:61] offset:1024 sc1\n" \
  "global_load_lds_dwordx4 %[d2], s[60:61] offset:2048 sc1\n" \
  "s_add_u32 s60, s60, 11776\n" \
  "s_addc_u32 s61, s61, 0\n" \
  ".Lentry%=:\n" \
  "v_mfma_f32_32x32x16_bf16 v[160:175], v[192:195], %[q2], 0\n" \
  "v_exp_f32 v128, v128\n" \
  "v_exp_f32 v129, v129\n" \
  "v_exp_f32 v130, v130\n" \
  "v_exp_f32 v131, v131\n" \
  "v_smfmac_f32_32x32x32_bf16 %[o3], v[202:205], v[184:191], %[idx]\n" \
  "ds_read_b128 v[202:205], %[av] offset:1536\n" \
  "v_exp_f32 v132, v132\n" \
  "v_exp_f32 v133, v133\n" \
  "v_exp_f32 v134, v134\n" \
  "v_exp_f32 v135, v135\n" \
  "v_mfma_f32_32x32x8_bf16 v[160:175], v[196:197], %[r2], v[160:175]\n" \
  "v_exp_f32 v144, v144\n" \
  "v_exp_f32 v145, v145\n" \
  "v_exp_f32 v146, v146\n" \
  "v_exp_f32 v147, v147\n" \
  "s_waitcnt lgkmcnt(1)\n" \
  "v_smfmac_f32_32x32x32_bf16 %[o0], v[198:201], v[128:135], %[idx]\n" \
  "v_exp_f32 v148, v148\n" \
  "v_exp_f32 v149, v149\n" \
  "v_exp_f32 v150, v150\n" \
  "v_exp_f32 v151, v151\n" \
  "v_mfma_f32_32x32x16_bf16 v[176:191], v[192:195], %[q3], 0\n" \
  "ds_read_b128 v[192:195], %[ak1] offset:0\n" \
  "v_exp_f32 v136, v136\n" \
  "v_exp_f32 v137, v137\n" \
  "v_exp_f32 v138, v138\n" \
  "v_exp_f32 v139, v139\n" \
  "v_smfmac_f32_32x32x32_bf16 %[o1], v[198:201], v[144:151], %[idx]\n" \
  "v_exp_f32 v140, v140\n" \
  "v_exp_f32 v141, v141\n" \
  "v_exp_f32 v142, v142\n" \
  "v_exp_f32 v143, v143\n" \
  "v_mfma_f32_32x32x8_bf16 v[176:191], v[196:197], %[r3], v[176:191]\n" \
  "ds_read_b64 v[196:197], %[ak2] offset:1024\n" \
  "v_exp_f32 v152, v152\n" \
  "v_exp_f32 v153, v153\n" \
  "v_exp_f32 v154, v154\n" \
  "v_exp_f32 v155, v155\n" \
  "s_waitcnt lgkmcnt(1)\n" \
  "v_smfmac_f32_32x32x32_bf16 %[o0], v[202:205], v[136:143], %[idx]\n" \
  "v_exp_f32 v156, v156\n" \
  "v_exp_f32 v157, v157\n" \
  "v_exp_f32 v158, v158\n" \
  "v_exp_f32 v159, v159\n" \
  "v_mfma_f32_32x32x16_bf16 v[128:143], v[192:195], %[q0], 0\n" \
  "v_exp_f32 v160, v160\n" \
  "v_exp_f32 v161, v161\n" \
  "v_exp_f32 v162, v162\n" \
  "v_exp_f32 v163, v163\n" \
  "v_smfmac_f32_32x32x32_bf16 %[o1], v[202:205], v[152:159], %[idx]\n" \
  "v_exp_f32 v164, v164\n" \
  "v_exp_f32 v165, v165\n" \
  "v_exp_f32 v166, v166\n" \
  "v_exp_f32 v167, v167\n" \
  "s_waitcnt lgkmcnt(0)\n" \
  "v_mfma_f32_32x32x8_bf16 v[128:143], v[196:197], %[r0], v[128:143]\n" \
  "v_exp_f32 v176, v176\n" \
  "v_exp_f32 v177, v177\n" \
  "v_exp_f32 v178, v178\n" \
  "v_exp_f32 v179, v179\n" \
  "v_smfmac_f32_32x32x32_bf16 %[o2], v[198:201], v[160:167], %[idx]\n" \
  "v_exp_f32 v180, v180\n" \
  "v_exp_f32 v181, v181\n" \
  "v_exp_f32 v182, v182\n" \
  "v_exp_f32 v183, v183\n" \
  "v_mfma_f32_32x32x16_bf16 v[144:159], v[192:195], %[q1], 0\n" \
  "v_exp_f32 v168, v168\n" \
  "v_exp_f32 v169, v169\n" \
  "v_exp_f32 v170, v170\n" \
  "v_exp_f32 v171, v171\n" \
  "v_smfmac_f32_32x32x32_bf16 %[o3], v[198:201], v[176:183], %[idx]\n" \
  "ds_read_b128 v[198:201], %[av] offset:2240\n" \
  "v_exp_f32 v172, v172\n" \
  "v_exp_f32 v173, v173\n" \
  "v_exp_f32 v174, v174\n" \
  "v_exp_f32 v175, v175\n" \
  "v_mfma_f32_32x32x8_bf16 v[144:159], v[196:197], %[r1], v[144:159]\n" \
  "v_exp_f32 v184, v184\n" \
  "v_exp_f32 v185, v185\n" \
  "v_exp_f32 v186, v186\n" \
  "v_exp_f32 v187, v187\n" \
  "v_smfmac_f32_32x32x32_bf16 %[o2], v[202:205], v[168:175], %[idx]\n" \
  "v_exp_f32 v188, v188\n" \
  "v_exp_f32 v189, v189\n" \
  "v_exp_f32 v190, v190\n" \
  "v_exp_f32 v191, v191\n" \
  "v_mfma_f32_32x32x16_bf16 v[160:175], v[192:195], %[q2], 0\n" \
  "v_exp_f32 v128, v128\n" \
  "v_exp_f32 v129, v129\n" \
  "v_exp_f32 v130, v130\n" \
  "v_exp_f32 v131, v131\n" \
  "v_smfmac_f32_32x32x32_bf16 %[o3], v[202:205], v[184:191], %[idx]\n" \
  "ds_read_b128 v[202:205], %[av] offset:4480\n" \
  "v_exp_f32 v132, v132\n" \
  "v_exp_f32 v133, v133\n" \
  "v_exp_f32 v134, v134\n" \
  "v_exp_f32 v135, v135\n" \
  "v_mfma_f32_32x32x8_bf16 v[160:175], v[196:197], %[r2], v[160:175]\n" \
  "v_exp_f32 v144, v144\n" \
  "v_exp_f32 v145, v145\n" \
  "v_exp_f32 v146, v146\n" \
  "v_exp_f32 v147, v147\n" \
  "s_waitcnt lgkmcnt(1)\n" \
  "v_smfmac_f32_32x32x32_bf16 %[o0], v[198:201], v[128:135], %[idx]\n" \
  "v_exp_f32 v148, v148\n" \
  "v_exp_f32 v149, v149\n" \
  "v_exp_f32 v150, v150\n" \
  "v_exp_f32 v151, v151\n" \
  "v_mfma_f32_32x32x16_bf16 v[176:191], v[192:195], %[q3], 0\n" \
  "ds_read_b128 v[192:195], %[ak1] offset:2944\n" \
  "v_exp_f32 v136, v136\n" \
  "v_exp_f32 v137, v137\n" \
  "v_exp_f32 v138, v138\n" \
  "v_exp_f32 v139, v139\n" \
  "v_smfmac_f32_32x32x32_bf16 %[o1], v[198:201], v[144:151], %[idx]\n" \
  "v_exp_f32 v140, v140\n" \
  "v_exp_f32 v141, v141\n" \
  "v_exp_f32 v142, v142\n" \
  "v_exp_f32 v143, v143\n" \
  "v_mfma_f32_32x32x8_bf16 v[176:191], v[196:197], %[r3], v[176:191]\n" \
  "s_mov_b32 exec_hi, 0x3ffff\n" \
  "s_mov_b32 m0, s64\n" \
  "s_add_u32 s64, s64, 6400\n" \
  "global_load_lds_dwordx4 %[wv], s[62:63]\n" \
  "s_add_u32 s62, s62, 51200\n" \
  "s_addc_u32 s63, s63, 0\n" \
  "s_mov_b32 m0, s64\n" \
  "s_add_u32 s64, s64, 6400\n" \
  "global_load_lds_dwordx4 %[wv], s[62:63]\n" \
  "s_add_u32 s62, s62, 51200\n" \
  "s_addc_u32 s63, s63, 0\n" \
  "s_mov_b32 exec_hi, -1\n" \
  "ds_read_b64 v[196:197], %[ak2] offset:3968\n" \
  "v_exp_f32 v152, v152\n" \
  "v_exp_f32 v153, v153\n" \
  "v_exp_f32 v154, v154\n" \
  "v_exp_f32 v155, v155\n" \
  "s_waitcnt lgkmcnt(1)\n" \
  "v_smfmac_f32_32x32x32_bf16 %[o0], v[202:205], v[136:143], %[idx]\n" \
  "v_exp_f32 v156, v156\n" \
  "v_exp_f32 v157, v157\n" \
  "v_exp_f32 v158, v158\n" \
  "v_exp_f32 v159, v159\n" \
  "v_mfma_f32_32x32x16_bf16 v[128:143], v[192:195], %[q0], 0\n" \
  "v_exp_f32 v160, v160\n" \
  "v_exp_f32 v161, v161\n" \
  "v_exp_f32 v162, v162\n" \
  "v_exp_f32 v163, v163\n" \
  "v_smfmac_f32_32x32x32_bf16 %[o1], v[202:205], v[152:159], %[idx]\n" \
  "v_exp_f32 v164, v164\n" \
  "v_exp_f32 v165, v165\n" \
  "v_exp_f32 v166, v166\n" \
  "v_exp_f32 v167, v167\n" \
  "s_waitcnt lgkmcnt(0)\n" \
  "v_mfma_f32_32x32x8_bf16 v[128:143], v[196:197], %[r0], v[128:143]\n" \
  "v_exp_f32 v176, v176\n" \
  "v_exp_f32 v177, v177\n" \
  "v_exp_f32 v178, v178\n" \
  "v_exp_f32 v179, v179\n" \
  "v_smfmac_f32_32x32x32_bf16 %[o2], v[198:201], v[160:167], %[idx]\n" \
  "v_exp_f32 v180, v180\n" \
  "v_exp_f32 v181, v181\n" \
  "v_exp_f32 v182, v182\n" \
  "v_exp_f32 v183, v183\n" \
  "v_mfma_f32_32x32x16_bf16 v[144:159], v[192:195], %[q1], 0\n" \
  "v_exp_f32 v168, v168\n" \
  "v_exp_f32 v169, v169\n" \
  "v_exp_f32 v170, v170\n" \
  "v_exp_f32 v171, v171\n" \
  "v_smfmac_f32_32x32x32_bf16 %[o3], v[198:201], v[176:183], %[idx]\n" \
  "ds_read_b128 v[198:201], %[av] offset:5184\n" \
  "v_exp_f32 v172, v172\n" \
  "v_exp_f32 v173, v173\n" \
  "v_exp_f32 v174, v174\n" \
  "v_exp_f32 v175, v175\n" \
  "v_mfma_f32_32x32x8_bf16 v[144:159], v[196:197], %[r1], v[144:159]\n" \
  "v_exp_f32 v184, v184\n" \
  "v_exp_f32 v185, v185\n" \
  "v_exp_f32 v186, v186\n" \
  "v_exp_f32 v187, v187\n" \
  "v_smfmac_f32_32x32x32_bf16 %[o2], v[202:205], v[168:175], %[idx]\n" \
  "v_exp_f32 v188, v188\n" \
  "v_exp_f32 v189, v189\n" \
  "v_exp_f32 v190, v190\n" \
  "v_exp_f32 v191, v191\n" \
  "v_mfma_f32_32x32x16_bf16 v[160:175], v[192:195], %[q2], 0\n" \
  "v_exp_f32 v128, v128\n" \
  "v_exp_f32 v129, v129\n" \
  "v_exp_f32 v130, v130\n" \
  "v_exp_f32 v131, v131\n" \
  "v_smfmac_f32_32x32x32_bf16 %[o3], v[202:205], v[184:191], %[idx]\n" \
  "ds_read_b128 v[202:205], %[av] offset:7424\n" \
  "v_exp_f32 v132, v132\n" \
  "v_exp_f32 v133, v133\n" \
  "v_exp_f32 v134, v134\n" \
  "v_exp_f32 v135, v135\n" \
  "v_mfma_f32_32x32x8_bf16 v[160:175], v[196:197], %[r2], v[160:175]\n" \
  "v_exp_f32 v144, v144\n" \
  "v_exp_f32 v145, v145\n" \
  "v_exp_f32 v146, v146\n" \
  "v_exp_f32 v147, v147\n" \
  "s_waitcnt lgkmcnt(1)\n" \
  "v_smfmac_f32_32x32x32_bf16 %[o0], v[198:201], v[128:135], %[idx]\n" \
  "v_exp_f32 v148, v148\n" \
  "v_exp_f32 v149, v149\n" \
  "v_exp_f32 v150, v150\n" \
  "v_exp_f32 v151, v151\n" \
  "v_mfma_f32_32x32x16_bf16 v[176:191], v[192:195], %[q3], 0\n" \
  "ds_read_b128 v[192:195], %[ak1] offset:5888\n" \
  "v_exp_f32 v136, v136\n" \
  "v_exp_f32 v137, v137\n" \
  "v_exp_f32 v138, v138\n" \
  "v_exp_f32 v139, v139\n" \
  "v_smfmac_f32_32x32x32_bf16 %[o1], v[198:201], v[144:151], %[idx]\n" \
  "v_exp_f32 v140, v140\n" \
  "v_exp_f32 v141, v141\n" \
  "v_exp_f32 v142, v142\n" \
  "v_exp_f32 v143, v143\n" \
  "v_mfma_f32_32x32x8_bf16 v[176:191], v[196:197], %[r3], v[176:191]\n" \
  "ds_read_b64 v[196:197], %[ak2] offset:6912\n" \
  "v_exp_f32 v152, v152\n" \
  "v_exp_f32 v153, v153\n" \
  "v_exp_f32 v154, v154\n" \
  "v_exp_f32 v155, v155\n" \
  "s_waitcnt lgkmcnt(1)\n" \
  "v_smfmac_f32_32x32x32_bf16 %[o0], v[202:205], v[136:143], %[idx]\n" \
  "v_exp_f32 v156, v156\n" \
  "v_exp_f32 v157, v157\n" \
  "v_exp_f32 v158, v158\n" \
  "v_exp_f32 v159, v159\n" \
  "v_mfma_f32_32x32x16_bf16 v[128:143], v[192:195], %[q0], 0\n" \
  "v_exp_f32 v160, v160\n" \
  "v_exp_f32 v161, v161\n" \
  "v_exp_f32 v162, v162\n" \
  "v_exp_f32 v163, v163\n" \
  "v_smfmac_f32_32x32x32_bf16 %[o1], v[202:205], v[152:159], %[idx]\n" \
  "v_exp_f32 v164, v164\n" \
  "v_exp_f32 v165, v165\n" \
  "v_exp_f32 v166, v166\n" \
  "v_exp_f32 v167, v167\n" \
  "s_waitcnt lgkmcnt(0)\n" \
  "v_mfma_f32_32x32x8_bf16 v[128:143], v[196:197], %[r0], v[128:143]\n" \
  "v_exp_f32 v176, v176\n" \
  "v_exp_f32 v177, v177\n" \
  "v_exp_f32 v178, v178\n" \
  "v_exp_f32 v179, v179\n" \
  "v_smfmac_f32_32x32x32_bf16 %[o2], v[198:201], v[160:167], %[idx]\n" \
  "v_exp_f32 v180, v180\n" \
  "v_exp_f32 v181, v181\n" \
  "v_exp_f32 v182, v182\n" \
  "v_exp_f32 v183, v183\n" \
  "v_mfma_f32_32x32x16_bf16 v[144:159], v[192:195], %[q1], 0\n" \
  "v_exp_f32 v168, v168\n" \
  "v_exp_f32 v169, v169\n" \
  "v_exp_f32 v170, v170\n" \
  "v_exp_f32 v171, v171\n" \
  "v_smfmac_f32_32x32x32_bf16 %[o3], v[198:201], v[176:183], %[idx]\n" \
  "ds_read_b128 v[198:201], %[av] offset:8128\n" \
  "v_exp_f32 v172, v172\n" \
  "v_exp_f32 v173, v173\n" \
  "v_exp_f32 v174, v174\n" \
  "v_exp_f32 v175, v175\n" \
  "v_mfma_f32_32x32x8_bf16 v[144:159], v[196:197], %[r1], v[144:159]\n" \
  "v_exp_f32 v184, v184\n" \
  "v_exp_f32 v185, v185\n" \
  "v_exp_f32 v186, v186\n" \
  "v_exp_f32 v187, v187\n" \
  "v_smfmac_f32_32x32x32_bf16 %[o2], v[202:205], v[168:175], %[idx]\n" \
  "v_exp_f32 v188, v188\n" \
  "v_exp_f32 v189, v189\n" \
  "v_exp_f32 v190, v190\n" \
  "v_exp_f32 v191, v191\n" \
  "v_mfma_f32_32x32x16_bf16 v[160:175], v[192:195], %[q2], 0\n" \
  "v_exp_f32 v128, v128\n" \
  "v_exp_f32 v129, v129\n" \
  "v_exp_f32 v130, v130\n" \
  "v_exp_f32 v131, v131\n" \
  "v_smfmac_f32_32x32x32_bf16 %[o3], v[202:205], v[184:191], %[idx]\n" \
  "ds_read_b128 v[202:205], %[av] offset:10368\n" \
  "v_exp_f32 v132, v132\n" \
  "v_exp_f32 v133, v133\n" \
  "v_exp_f32 v134, v134\n" \
  "v_exp_f32 v135, v135\n" \
  "v_mfma_f32_32x32x8_bf16 v[160:175], v[196:197], %[r2], v[160:175]\n" \
  "v_exp_f32 v144, v144\n" \
  "v_exp_f32 v145, v145\n" \
  "v_exp_f32 v146, v146\n" \
  "v_exp_f32 v147, v147\n" \
  "s_waitcnt lgkmcnt(1)\n" \
  "v_smfmac_f32_32x32x32_bf16 %[o0], v[198:201], v[128:135], %[idx]\n" \
  "v_exp_f32 v148, v148\n" \
  "v_exp_f32 v149, v149\n" \
  "v_exp_f32 v150, v150\n" \
  "v_exp_f32 v151, v151\n" \
  "v_mfma_f32_32x32x16_bf16 v[176:191], v[192:195], %[q3], 0\n" \
  "ds_read_b128 v[192:195], %[ak1] offset:8832\n" \
  "v_exp_f32 v136, v136\n" \
  "v_exp_f32 v137, v137\n" \
  "v_exp_f32 v138, v138\n" \
  "v_exp_f32 v139, v139\n" \
  "v_smfmac_f32_32x32x32_bf16 %[o1], v[198:201], v[144:151], %[idx]\n" \
  "v_exp_f32 v140, v140\n" \
  "v_exp_f32 v141, v141\n" \
  "v_exp_f32 v142, v142\n" \
  "v_exp_f32 v143, v143\n" \
  "v_mfma_f32_32x32x8_bf16 v[176:191], v[196:197], %[r3], v[176:191]\n" \
  "ds_read_b64 v[196:197], %[ak2] offset:9856\n" \
  "v_exp_f32 v152, v152\n" \
  "v_exp_f32 v153, v153\n" \
  "v_exp_f32 v154, v154\n" \
  "v_exp_f32 v155, v155\n" \
  "s_waitcnt lgkmcnt(1)\n" \
  "v_smfmac_f32_32x32x32_bf16 %[o0], v[202:205], v[136:143], %[idx]\n" \
  "v_exp_f32 v156, v156\n" \
  "v_exp_f32 v157, v157\n" \
  "v_exp_f32 v158, v158\n" \
  "v_exp_f32 v159, v159\n" \
  "v_mfma_f32_32x32x16_bf16 v[128:143], v[192:195], %[q0], 0\n" \
  "v_exp_f32 v160, v160\n" \
  "v_exp_f32 v161, v161\n" \
  "v_exp_f32 v162, v162\n" \
  "v_exp_f32 v163, v163\n" \
  "v_smfmac_f32_32x32x32_bf16 %[o1], v[202:205], v[152:159], %[idx]\n" \
  "v_exp_f32 v164, v164\n" \
  "v_exp_f32 v165, v165\n" \
  "v_exp_f32 v166, v166\n" \
  "v_exp_f32 v167, v167\n" \
  "s_waitcnt lgkmcnt(0)\n" \
  "v_mfma_f32_32x32x8_bf16 v[128:143], v[196:197], %[r0], v[128:143]\n" \
  "v_exp_f32 v176, v176\n" \
  "v_exp_f32 v177, v177\n" \
  "v_exp_f32 v178, v178\n" \
  "v_exp_f32 v179, v179\n" \
  "v_smfmac_f32_32x32x32_bf16 %[o2], v[198:201], v[160:167], %[idx]\n" \
  "v_exp_f32 v180, v180\n" \
  "v_exp_f32 v181, v181\n" \
  "v_exp_f32 v182, v182\n" \
  "v_exp_f32 v183, v183\n" \
  "v_mfma_f32_32x32x16_bf16 v[144:159], v[192:195], %[q1], 0\n" \
  "v_exp_f32 v168, v168\n" \
  "v_exp_f32 v169, v169\n" \
  "v_exp_f32 v170, v170\n" \
  "v_exp_f32 v171, v171\n" \
  "v_smfmac_f32_32x32x32_bf16 %[o3], v[198:201], v[176:183], %[idx]\n" \
  "ds_read_b128 v[198:201], %[av] offset:11072\n" \
  "v_exp_f32 v172, v172\n" \
  "v_exp_f32 v173, v173\n" \
  "v_exp_f32 v174, v174\n" \
  "v_exp_f32 v175, v175\n" \
  "v_mfma_f32_32x32x8_bf16 v[144:159], v[196:197], %[r1], v[144:159]\n" \
  "v_exp_f32 v184, v184\n" \
  "v_exp_f32 v185, v185\n" \
  "v_exp_f32 v186, v186\n" \
  "v_exp_f32 v187, v187\n" \
  "v_smfmac_f32_32x32x32_bf16 %[o2], v[202:205], v[168:175], %[idx]\n" \
  "v_exp_f32 v188, v188\n" \
  "v_exp_f32 v189, v189\n" \
  "v_exp_f32 v190, v190\n" \
  "v_exp_f32 v191, v191\n" \
  "s_waitcnt vmcnt(0) lgkmcnt(0)\n" \
  "s_barrier\n" \
  "s_cmp_eq_u32 %[cnt], 1\n" \
  "s_cbranch_scc1 .Lskip%=\n" \
  "s_mov_b32 m0, %[m0a]\n" \
  "s_nop 0\n" \
  "global_load_lds_dwordx4 %[d0], s[60:61] sc1\n" \
  "global_load_lds_dwordx4 %[d1], s[60:61] offset:1024 sc1\n" \
  "global_load_lds_dwordx4 %[d2], s[60:61] offset:2048 sc1\n" \
  "s_add_u32 s60, s60, 11776\n" \
  "s_addc_u32 s61, s61, 0\n" \
  ".Lskip%=:\n" \
  "v_mfma_f32_32x32x16_bf16 v[160:175], v[192:195], %[q2], 0\n" \
  "v_exp_f32 v128, v128\n" \
  "v_exp_f32 v129, v129\n" \
  "v_exp_f32 v130, v130\n" \
  "v_exp_f32 v131, v131\n" \
  "v_smfmac_f32_32x32x32_bf16 %[o3], v[202:205], v[184:191], %[idx]\n" \
  "ds_read_b128 v[202:205], %[av] offset:25088\n" \
  "v_exp_f32 v132, v132\n" \
  "v_exp_f32 v133, v133\n" \
  "v_exp_f32 v134, v134\n" \
  "v_exp_f32 v135, v135\n" \
  "v_mfma_f32_32x32x8_bf16 v[160:175], v[196:197], %[r2], v[160:175]\n" \
  "v_exp_f32 v144, v144\n" \
  "v_exp_f32 v145, v145\n" \
  "v_exp_f32 v146, v146\n" \
  "v_exp_f32 v147, v147\n" \
  "s_waitcnt lgkmcnt(1)\n" \
  "v_smfmac_f32_32x32x32_bf16 %[o0], v[198:201], v[128:135], %[idx]\n" \
  "v_exp_f32 v148, v148\n" \
  "v_exp_f32 v149, v149\n" \
  "v_exp_f32 v150, v150\n" \
  "v_exp_f32 v151, v151\n" \
  "v_mfma_f32_32x32x16_bf16 v[176:191], v[192:195], %[q3], 0\n" \
  "ds_read_b128 v[192:195], %[ak1] offset:23552\n" \
  "v_exp_f32 v136, v136\n" \
  "v_exp_f32 v137, v137\n" \
  "v_exp_f32 v138, v138\n" \
  "v_exp_f32 v139, v139\n" \
  "v_smfmac_f32_32x32x32_bf16 %[o1], v[198:201], v[144:151], %[idx]\n" \
  "v_exp_f32 v140, v140\n" \
  "v_exp_f32 v141, v141\n" \
  "v_exp_f32 v142, v142\n" \
  "v_exp_f32 v143, v143\n" \
  "v_mfma_f32_32x32x8_bf16 v[176:191], v[196:197], %[r3], v[176:191]\n" \
  "ds_read_b64 v[196:197], %[ak2] offset:24576\n" \
  "v_exp_f32 v152, v152\n" \
  "v_exp_f32 v153, v153\n" \
  "v_exp_f32 v154, v154\n" \
  "v_exp_f32 v155, v155\n" \
  "s_waitcnt lgkmcnt(1)\n" \
  "v_smfmac_f32_32x32x32_bf16 %[o0], v[202:205], v[136:143], %[idx]\n" \
  "v_exp_f32 v156, v156\n" \
  "v_exp_f32 v157, v157\n" \
  "v_exp_f32 v158, v158\n" \
  "v_exp_f32 v159, v159\n" \
  "v_mfma_f32_32x32x16_bf16 v[128:143], v[192:195], %[q0], 0\n" \
  "v_exp_f32 v160, v160\n" \
  "v_exp_f32 v161, v161\n" \
  "v_exp_f32 v162, v162\n" \
  "v_exp_f32 v163, v163\n" \
  "v_smfmac_f32_32x32x32_bf16 %[o1], v[202:205], v[152:159], %[idx]\n" \
  "v_exp_f32 v164, v164\n" \
  "v_exp_f32 v165, v165\n" \
  "v_exp_f32 v166, v166\n" \
  "v_exp_f32 v167, v167\n" \
  "s_waitcnt lgkmcnt(0)\n" \
  "v_mfma_f32_32x32x8_bf16 v[128:143], v[196:197], %[r0], v[128:143]\n" \
  "v_exp_f32 v176, v176\n" \
  "v_exp_f32 v177, v177\n" \
  "v_exp_f32 v178, v178\n" \
  "v_exp_f32 v179, v179\n" \
  "v_smfmac_f32_32x32x32_bf16 %[o2], v[198:201], v[160:167], %[idx]\n" \
  "v_exp_f32 v180, v180\n" \
  "v_exp_f32 v181, v181\n" \
  "v_exp_f32 v182, v182\n" \
  "v_exp_f32 v183, v183\n" \
  "v_mfma_f32_32x32x16_bf16 v[144:159], v[192:195], %[q1], 0\n" \
  "v_exp_f32 v168, v168\n" \
  "v_exp_f32 v169, v169\n" \
  "v_exp_f32 v170, v170\n" \
  "v_exp_f32 v171, v171\n" \
  "v_smfmac_f32_32x32x32_bf16 %[o3], v[198:201], v[176:183], %[idx]\n" \
  "ds_read_b128 v[198:201], %[av] offset:25792\n" \
  "v_exp_f32 v172, v172\n" \
  "v_exp_f32 v173, v173\n" \
  "v_exp_f32 v174, v174\n" \
  "v_exp_f32 v175, v175\n" \
  "v_mfma_f32_32x32x8_bf16 v[144:159], v[196:197], %[r1], v[144:159]\n" \
  "v_exp_f32 v184, v184\n" \
  "v_exp_f32 v185, v185\n" \
  "v_exp_f32 v186, v186\n" \
  "v_exp_f32 v187, v187\n" \
  "v_smfmac_f32_32x32x32_bf16 %[o2], v[202:205], v[168:175], %[idx]\n" \
  "v_exp_f32 v188, v188\n" \
  "v_exp_f32 v189, v189\n" \
  "v_exp_f32 v190, v190\n" \
  "v_exp_f32 v191, v191\n" \
  "v_mfma_f32_32x32x16_bf16 v[160:175], v[192:195], %[q2], 0\n" \
  "v_exp_f32 v128, v128\n" \
  "v_exp_f32 v129, v129\n" \
  "v_exp_f32 v130, v130\n" \
  "v_exp_f32 v131, v131\n" \
  "v_smfmac_f32_32x32x32_bf16 %[o3], v[202:205], v[184:191], %[idx]\n" \
  "ds_read_b128 v[202:205], %[av] offset:28032\n" \
  "v_exp_f32 v132, v132\n" \
  "v_exp_f32 v133, v133\n" \
  "v_exp_f32 v134, v134\n" \
  "v_exp_f32 v135, v135\n" \
  "v_mfma_f32_32x32x8_bf16 v[160:175], v[196:197], %[r2], v[160:175]\n" \
  "v_exp_f32 v144, v144\n" \
  "v_exp_f32 v145, v145\n" \
  "v_exp_f32 v146, v146\n" \
  "v_exp_f32 v147, v147\n" \
  "s_waitcnt lgkmcnt(1)\n" \
  "v_smfmac_f32_32x32x32_bf16 %[o0], v[198:201], v[128:135], %[idx]\n" \
  "v_exp_f32 v148, v148\n" \
  "v_exp_f32 v149, v149\n" \
  "v_exp_f32 v150, v150\n" \
  "v_exp_f32 v151, v151\n" \
  "v_mfma_f32_32x32x16_bf16 v[176:191], v[192:195], %[q3], 0\n" \
  "ds_read_b128 v[192:195], %[ak1] offset:26496\n" \
  "v_exp_f32 v136, v136\n" \
  "v_exp_f32 v137, v137\n" \
  "v_exp_f32 v138, v138\n" \
  "v_exp_f32 v139, v139\n" \
  "v_smfmac_f32_32x32x32_bf16 %[o1], v[198:201], v[144:151], %[idx]\n" \
  "v_exp_f32 v140, v140\n" \
  "v_exp_f32 v141, v141\n" \
  "v_exp_f32 v142, v142\n" \
  "v_exp_f32 v143, v143\n" \
  "v_mfma_f32_32x32x8_bf16 v[176:191], v[196:197], %[r3], v[176:191]\n" \
  "s_mov_b32 exec_hi, 0x3ffff\n" \
  "s_mov_b32 m0, s64\n" \
  "s_add_u32 s64, s64, 6400\n" \
  "global_load_lds_dwordx4 %[wv], s[62:63]\n" \
  "s_add_u32 s62, s62, 51200\n" \
  "s_addc_u32 s63, s63, 0\n" \
  "s_mov_b32 m0, s64\n" \
  "s_add_u32 s64, s64, 6400\n" \
  "global_load_lds_dwordx4 %[wv], s[62:63]\n" \
  "s_add_u32 s62, s62, 51200\n" \
  "s_addc_u32 s63, s63, 0\n" \
  "s_mov_b32 exec_hi, -1\n" \
  "ds_read_b64 v[196:197], %[ak2] offset:27520\n" \
  "v_exp_f32 v152, v152\n" \
  "v_exp_f32 v153, v153\n" \
  "v_exp_f32 v154, v154\n" \
  "v_exp_f32 v155, v155\n" \
  "s_waitcnt lgkmcnt(1)\n" \
  "v_smfmac_f32_32x32x32_bf16 %[o0], v[202:205], v[136:143], %[idx]\n" \
  "v_exp_f32 v156, v156\n" \
  "v_exp_f32 v157, v157\n" \
  "v_exp_f32 v158, v158\n" \
  "v_exp_f32 v159, v159\n" \
  "v_mfma_f32_32x32x16_bf16 v[128:143], v[192:195], %[q0], 0\n" \
  "v_exp_f32 v160, v160\n" \
  "v_exp_f32 v161, v161\n" \
  "v_exp_f32 v162, v162\n" \
  "v_exp_f32 v163, v163\n" \
  "v_smfmac_f32_32x32x32_bf16 %[o1], v[202:205], v[152:159], %[idx]\n" \
  "v_exp_f32 v164, v164\n" \
  "v_exp_f32 v165, v165\n" \
  "v_exp_f32 v166, v166\n" \
  "v_exp_f32 v167, v167\n" \
  "s_waitcnt lgkmcnt(0)\n" \
  "v_mfma_f32_32x32x8_bf16 v[128:143], v[196:197], %[r0], v[128:143]\n" \
  "v_exp_f32 v176, v176\n" \
  "v_exp_f32 v177, v177\n" \
  "v_exp_f32 v178, v178\n" \
  "v_exp_f32 v179, v179\n" \
  "v_smfmac_f32_32x32x32_bf16 %[o2], v[198:201], v[160:167], %[idx]\n" \
  "v_exp_f32 v180, v180\n" \
  "v_exp_f32 v181, v181\n" \
  "v_exp_f32 v182, v182\n" \
  "v_exp_f32 v183, v183\n" \
  "v_mfma_f32_32x32x16_bf16 v[144:159], v[192:195], %[q1], 0\n" \
  "v_exp_f32 v168, v168\n" \
  "v_exp_f32 v169, v169\n" \
  "v_exp_f32 v170, v170\n" \
  "v_exp_f32 v171, v171\n" \
  "v_smfmac_f32_32x32x32_bf16 %[o3], v[198:201], v[176:183], %[idx]\n" \
  "ds_read_b128 v[198:201], %[av] offset:28736\n" \
  "v_exp_f32 v172, v172\n" \
  "v_exp_f32 v173, v173\n" \
  "v_exp_f32 v174, v174\n" \
  "v_exp_f32 v175, v175\n" \
  "v_mfma_f32_32x32x8_bf16 v[144:159], v[196:197], %[r1], v[144:159]\n" \
  "v_exp_f32 v184, v184\n" \
  "v_exp_f32 v185, v185\n" \
  "v_exp_f32 v186, v186\n" \
  "v_exp_f32 v187, v187\n" \
  "v_smfmac_f32_32x32x32_bf16 %[o2], v[202:205], v[168:175], %[idx]\n" \
  "v_exp_f32 v188, v188\n" \
  "v_exp_f32 v189, v189\n" \
  "v_exp_f32 v190, v190\n" \
  "v_exp_f32 v191, v191\n" \
  "v_mfma_f32_32x32x16_bf16 v[160:175], v[192:195], %[q2], 0\n" \
  "v_exp_f32 v128, v128\n" \
  "v_exp_f32 v129, v129\n" \
  "v_exp_f32 v130, v130\n" \
  "v_exp_f32 v131, v131\n" \
  "v_smfmac_f32_32x32x32_bf16 %[o3], v[202:205], v[184:191], %[idx]\n" \
  "ds_read_b128 v[202:205], %[av] offset:30976\n" \
  "v_exp_f32 v132, v132\n" \
  "v_exp_f32 v133, v133\n" \
  "v_exp_f32 v134, v134\n" \
  "v_exp_f32 v135, v135\n" \
  "v_mfma_f32_32x32x8_bf16 v[160:175], v[196:197], %[r2], v[160:175]\n" \
  "v_exp_f32 v144, v144\n" \
  "v_exp_f32 v145, v145\n" \
  "v_exp_f32 v146, v146\n" \
  "v_exp_f32 v147, v147\n" \
  "s_waitcnt lgkmcnt(1)\n" \
  "v_smfmac_f32_32x32x32_bf16 %[o0], v[198:201], v[128:135], %[idx]\n" \
  "v_exp_f32 v148, v148\n" \
  "v_exp_f32 v149, v149\n" \
  "v_exp_f32 v150, v150\n" \
  "v_exp_f32 v151, v151\n" \
  "v_mfma_f32_32x32x16_bf16 v[176:191], v[192:195], %[q3], 0\n" \
  "ds_read_b128 v[192:195], %[ak1] offset:29440\n" \
  "v_exp_f32 v136, v136\n" \
  "v_exp_f32 v137, v137\n" \
  "v_exp_f32 v138, v138\n" \
  "v_exp_f32 v139, v139\n" \
  "v_smfmac_f32_32x32x32_bf16 %[o1], v[198:201], v[144:151], %[idx]\n" \
  "v_exp_f32 v140, v140\n" \
  "v_exp_f32 v141, v141\n" \
  "v_exp_f32 v142, v142\n" \
  "v_exp_f32 v143, v143\n" \
  "v_mfma_f32_32x32x8_bf16 v[176:191], v[196:197], %[r3], v[176:191]\n" \
  "ds_read_b64 v[196:197], %[ak2] offset:30464\n" \
  "v_exp_f32 v152, v152\n" \
  "v_exp_f32 v153, v153\n" \
  "v_exp_f32 v154, v154\n" \
  "v_exp_f32 v155, v155\n" \
  "s_waitcnt lgkmcnt(1)\n" \
  "v_smfmac_f32_32x32x32_bf16 %[o0], v[202:205], v[136:143], %[idx]\n" \
  "v_exp_f32 v156, v156\n" \
  "v_exp_f32 v157, v157\n" \
  "v_exp_f32 v158, v158\n" \
  "v_exp_f32 v159, v159\n" \
  "v_mfma_f32_32x32x16_bf16 v[128:143], v[192:195], %[q0], 0\n" \
  "v_exp_f32 v160, v160\n" \
  "v_exp_f32 v161, v161\n" \
  "v_exp_f32 v162, v162\n" \
  "v_exp_f32 v163, v163\n" \
  "v_smfmac_f32_32x32x32_bf16 %[o1], v[202:205], v[152:159], %[idx]\n" \
  "v_exp_f32 v164, v164\n" \
  "v_exp_f32 v165, v165\n" \
  "v_exp_f32 v166, v166\n" \
  "v_exp_f32 v167, v167\n" \
  "s_waitcnt lgkmcnt(0)\n" \
  "v_mfma_f32_32x32x8_bf16 v[128:143], v[196:197], %[r0], v[128:143]\n" \
  "v_exp_f32 v176, v176\n" \
  "v_exp_f32 v177, v177\n" \
  "v_exp_f32 v178, v178\n" \
  "v_exp_f32 v179, v179\n" \
  "v_smfmac_f32_32x32x32_bf16 %[o2], v[198:201], v[160:167], %[idx]\n" \
  "v_exp_f32 v180, v180\n" \
  "v_exp_f32 v181, v181\n" \
  "v_exp_f32 v182, v182\n" \
  "v_exp_f32 v183, v183\n" \
  "v_mfma_f32_32x32x16_bf16 v[144:159], v[192:195], %[q1], 0\n" \
  "v_exp_f32 v168, v168\n" \
  "v_exp_f32 v169, v169\n" \
  "v_exp_f32 v170, v170\n" \
  "v_exp_f32 v171, v171\n" \
  "v_smfmac_f32_32x32x32_bf16 %[o3], v[198:201], v[176:183], %[idx]\n" \
  "ds_read_b128 v[198:201], %[av] offset:31680\n" \
  "v_exp_f32 v172, v172\n" \
  "v_exp_f32 v173, v173\n" \
  "v_exp_f32 v174, v174\n" \
  "v_exp_f32 v175, v175\n" \
  "v_mfma_f32_32x32x8_bf16 v[144:159], v[196:197], %[r1], v[144:159]\n" \
  "v_exp_f32 v184, v184\n" \
  "v_exp_f32 v185, v185\n" \
  "v_exp_f32 v186, v186\n" \
  "v_exp_f32 v187, v187\n" \
  "v_smfmac_f32_32x32x32_bf16 %[o2], v[202:205], v[168:175], %[idx]\n" \
  "v_exp_f32 v188, v188\n" \
  "v_exp_f32 v189, v189\n" \
  "v_exp_f32 v190, v190\n" \
  "v_exp_f32 v191, v191\n" \
  "v_mfma_f32_32x32x16_bf16 v[160:175], v[192:195], %[q2], 0\n" \
  "v_exp_f32 v128, v128\n" \
  "v_exp_f32 v129, v129\n" \
  "v_exp_f32 v130, v130\n" \
  "v_exp_f32 v131, v131\n" \
  "v_smfmac_f32_32x32x32_bf16 %[o3], v[202:205], v[184:191], %[idx]\n" \
  "ds_read_b128 v[202:205], %[av] offset:33920\n" \
  "v_exp_f32 v132, v132\n" \
  "v_exp_f32 v133, v133\n" \
  "v_exp_f32 v134, v134\n" \
  "v_exp_f32 v135, v135\n" \
  "v_mfma_f32_32x32x8_bf16 v[160:175], v[196:197], %[r2], v[160:175]\n" \
  "v_exp_f32 v144, v144\n" \
  "v_exp_f32 v145, v145\n" \
  "v_exp_f32 v146, v146\n" \
  "v_exp_f32 v147, v147\n" \
  "s_waitcnt lgkmcnt(1)\n" \
  "v_smfmac_f32_32x32x32_bf16 %[o0], v[198:201], v[128:135], %[idx]\n" \
  "v_exp_f32 v148, v148\n" \
  "v_exp_f32 v149, v149\n" \
  "v_exp_f32 v150, v150\n" \
  "v_exp_f32 v151, v151\n" \
  "v_mfma_f32_32x32x16_bf16 v[176:191], v[192:195], %[q3], 0\n" \
  "ds_read_b128 v[192:195], %[ak1] offset:32384\n" \
  "v_exp_f32 v136, v136\n" \
  "v_exp_f32 v137, v137\n" \
  "v_exp_f32 v138, v138\n" \
  "v_exp_f32 v139, v139\n" \
  "v_smfmac_f32_32x32x32_bf16 %[o1], v[198:201], v[144:151], %[idx]\n" \
  "v_exp_f32 v140, v140\n" \
  "v_exp_f32 v141, v141\n" \
  "v_exp_f32 v142, v142\n" \
  "v_exp_f32 v143, v143\n" \
  "v_mfma_f32_32x32x8_bf16 v[176:191], v[196:197], %[r3], v[176:191]\n" \
  "ds_read_b64 v[196:197], %[ak2] offset:33408\n" \
  "v_exp_f32 v152, v152\n" \
  "v_exp_f32 v153, v153\n" \
  "v_exp_f32 v154, v154\n" \
  "v_exp_f32 v155, v155\n" \
  "s_waitcnt lgkmcnt(1)\n" \
  "v_smfmac_f32_32x32x32_bf16 %[o0], v[202:205], v[136:143], %[idx]\n" \
  "v_exp_f32 v156, v156\n" \
  "v_exp_f32 v157, v157\n" \
  "v_exp_f32 v158, v158\n" \
  "v_exp_f32 v159, v159\n" \
  "v_mfma_f32_32x32x16_bf16 v[128:143], v[192:195], %[q0], 0\n" \
  "v_exp_f32 v160, v160\n" \
  "v_exp_f32 v161, v161\n" \
  "v_exp_f32 v162, v162\n" \
  "v_exp_f32 v163, v163\n" \
  "v_smfmac_f32_32x32x32_bf16 %[o1], v[202:205], v[152:159], %[idx]\n" \
  "v_exp_f32 v164, v164\n" \
  "v_exp_f32 v165, v165\n" \
  "v_exp_f32 v166, v166\n" \
  "v_exp_f32 v167, v167\n" \
  "s_waitcnt lgkmcnt(0)\n" \
  "v_mfma_f32_32x32x8_bf16 v[128:143], v[196:197], %[r0], v[128:143]\n" \
  "v_exp_f32 v176, v176\n" \
  "v_exp_f32 v177, v177\n" \
  "v_exp_f32 v178, v178\n" \
  "v_exp_f32 v179, v179\n" \
  "v_smfmac_f32_32x32x32_bf16 %[o2], v[198:201], v[160:167], %[idx]\n" \
  "v_exp_f32 v180, v180\n" \
  "v_exp_f32 v181, v181\n" \
  "v_exp_f32 v182, v182\n" \
  "v_exp_f32 v183, v183\n" \
  "v_mfma_f32_32x32x16_bf16 v[144:159], v[192:195], %[q1], 0\n" \
  "v_exp_f32 v168, v168\n" \
  "v_exp_f32 v169, v169\n" \
  "v_exp_f32 v170, v170\n" \
  "v_exp_f32 v171, v171\n" \
  "v_smfmac_f32_32x32x32_bf16 %[o3], v[198:201], v[176:183], %[idx]\n" \
  "ds_read_b128 v[198:201], %[av] offset:34624\n" \
  "v_exp_f32 v172, v172\n" \
  "v_exp_f32 v173, v173\n" \
  "v_exp_f32 v174, v174\n" \
  "v_exp_f32 v175, v175\n" \
  "v_mfma_f32_32x32x8_bf16 v[144:159], v[196:197], %[r1], v[144:159]\n" \
  "v_exp_f32 v184, v184\n" \
  "v_exp_f32 v185, v185\n" \
  "v_exp_f32 v186, v186\n" \
  "v_exp_f32 v187, v187\n" \
  "v_smfmac_f32_32x32x32_bf16 %[o2], v[202:205], v[168:175], %[idx]\n" \
  "v_exp_f32 v188, v188\n" \
  "v_exp_f32 v189, v189\n" \
  "v_exp_f32 v190, v190\n" \
  "v_exp_f32 v191, v191\n" \
  "s_sub_u32 %[cnt], %[cnt], 1\n" \
  "s_cmp_lg_u32 %[cnt], 0\n" \
  "s_cbranch_scc1 .Lloop%=\n" \
  "s_nop 1\n" \
  "v_smfmac_f32_32x32x32_bf16 %[o3], v[202:205], v[184:191], %[idx]\n" \
  "s_waitcnt lgkmcnt(0)\n" \
  "s_mov_b32 m0, %[sv]\n" \
  "s_nop 15\n" \

#define ATTN_ASM_Y \
  "s_mov_b32 %[sv], m0\n" \
  "s_mov_b64 s[60:61], %[dma]\n" \
  "v_mov_b64 v[192:193], %[k1a]\n" \
  "v_mov_b64 v[194:195], %[k1b]\n" \
  "v_mov_b64 v[196:197], %[k2]\n" \
  "v_mov_b64 v[198:199], %[v0a]\n" \
  "v_mov_b64 v[200:201], %[v0b]\n" \
  "v_mov_b32 v202, 0\n" \
  "v_mov_b32 v203, 0\n" \
  "v_mov_b32 v204, 0\n" \
  "v_mov_b32 v205, 0\n" \
  "v_mov_b32 v184, 0\n" \
  "v_mov_b32 v185, 0\n" \
  "v_mov_b32 v186, 0\n" \
  "v_mov_b32 v187, 0\n" \
  "v_mov_b32 v188, 0\n" \
  "v_mov_b32 v189, 0\n" \
  "v_mov_b32 v190, 0\n" \
  "v_mov_b32 v191, 0\n" \
  "s_nop 1\n" \
  "v_mfma_f32_32x32x16_bf16 v[128:143], v[192:195], %[q0], 0\n" \
  "v_mfma_f32_32x32x16_bf16 v[144:159], v[192:195], %[q1], 0\n" \
  "v_mfma_f32_32x32x8_bf16 v[128:143], v[196:197], %[r0], v[128:143]\n" \
  "v_mfma_f32_32x32x8_bf16 v[144:159], v[196:197], %[r1], v[144:159]\n" \
  "s_waitcnt vmcnt(0)\n" \
  "s_barrier\n" \
  "s_mov_b32 m0, %[m0b]\n" \
  "s_nop 0\n" \
  "global_load_lds_dwordx4 %[d0], s[60:61] sc1\n" \
  "global_load_lds_dwordx4 %[d1], s[60:61] offset:1024 sc1\n" \
  "global_load_lds_dwordx4 %[d2], s[60:61] offset:2048 sc1\n" \
  "s_add_u32 s60, s60, 11776\n" \
  "s_addc_u32 s61, s61, 0\n" \
  "s_mov_b64 s[62:63], %[wsrc]\n" \
  "s_mov_b32 s64, %[wdst]\n" \
  "s_nop 7\n" \
  "s_branch .Lentry%=\n" \
  ".Lloop%=:\n" \
  "s_waitcnt vmcnt(0) lgkmcnt(0)\n" \
  "s_barrier\n" \
  "s_mov_b32 m0, %[m0b]\n" \
  "s_nop 0\n" \
  "global_load_lds_dwordx4 %[d0], s[60:61] sc1\n" \
  "global_load_lds_dwordx4 %[d1], s[60:61] offset:1024 sc1\n" \
  "global_load_lds_dwordx4 %[d2], s[60:61] offset:2048 sc1\n" \
  "s_add_u32 s60, s60, 11776\n" \
  "s_addc_u32 s61, s61, 0\n" \
  ".Lentry%=:\n" \
  "s_setprio 1\n" \
  "v_mfma_f32_32x32x16_bf16 v[160:175], v[192:195], %[q2], 0\n" \
  "v_exp_f32 v128, v128\n" \
  "v_exp_f32 v129, v129\n" \
  "v_exp_f32 v130, v130\n" \
  "v_exp_f32 v131, v131\n" \
  "v_smfmac_f32_32x32x32_bf16 %[o3], v[202:205], v[184:191], %[idx]\n" \
  "ds_read_b128 v[202:205], %[av] offset:1536\n" \
  "v_exp_f32 v132, v132\n" \
  "v_exp_f32 v133, v133\n" \
  "v_exp_f32 v134, v134\n" \
  "v_exp_f32 v135, v135\n" \
  "v_mfma_f32_32x32x8_bf16 v[160:175], v[196:197], %[r2], v[160:175]\n" \
  "v_exp_f32 v144, v144\n" \
  "v_exp_f32 v145, v145\n" \
  "v_exp_f32 v146, v146\n" \
  "v_exp_f32 v147, v147\n" \
  "s_waitcnt lgkmcnt(1)\n" \
  "v_smfmac_f32_32x32x32_bf16 %[o0], v[198:201], v[128:135], %[idx]\n" \
  "v_exp_f32 v148, v148\n" \
  "v_exp_f32 v149, v149\n" \
  "v_exp_f32 v150, v150\n" \
  "v_exp_f32 v151, v151\n" \
  "v_mfma_f32_32x32x16_bf16 v[176:191], v[192:195], %[q3], 0\n" \
  "ds_read_b128 v[192:195], %[ak1] offset:0\n" \
  "v_exp_f32 v136, v136\n" \
  "v_exp_f32 v137, v137\n" \
  "v_exp_f32 v138, v138\n" \
  "v_exp_f32 v139, v139\n" \
  "v_smfmac_f32_32x32x32_bf16 %[o1], v[198:201], v[144:151], %[idx]\n" \
  "v_exp_f32 v140, v140\n" \
  "v_exp_f32 v141, v141\n" \
  "v_exp_f32 v142, v142\n" \
  "v_exp_f32 v143, v143\n" \
  "v_mfma_f32_32x32x8_bf16 v[176:191], v[196:197], %[r3], v[176:191]\n" \
  "ds_read_b64 v[196:197], %[ak2] offset:1024\n" \
  "v_exp_f32 v152, v152\n" \
  "v_exp_f32 v153, v153\n" \
  "v_exp_f32 v154, v154\n" \
  "v_exp_f32 v155, v155\n" \
  "s_waitcnt lgkmcnt(1)\n" \
  "v_smfmac_f32_32x32x32_bf16 %[o0], v[202:205], v[136:143], %[idx]\n" \
  "v_exp_f32 v156, v156\n" \
  "v_exp_f32 v157, v157\n" \
  "v_exp_f32 v158, v158\n" \
  "v_exp_f32 v159, v159\n" \
  "v_mfma_f32_32x32x16_bf16 v[128:143], v[192:195], %[q0], 0\n" \
  "v_exp_f32 v160, v160\n" \
  "v_exp_f32 v161, v161\n" \
  "v_exp_f32 v162, v162\n" \
  "v_exp_f32 v163, v163\n" \
  "v_smfmac_f32_32x32x32_bf16 %[o1], v[202:205], v[152:159], %[idx]\n" \
  "v_exp_f32 v164, v164\n" \
  "v_exp_f32 v165, v165\n" \
  "v_exp_f32 v166, v166\n" \
  "v_exp_f32 v167, v167\n" \
  "s_waitcnt lgkmcnt(0)\n" \
  "v_mfma_f32_32x32x8_bf16 v[128:143], v[196:197], %[r0], v[128:143]\n" \
  "s_setprio 0\n" \
  "v_exp_f32 v176, v176\n" \
  "v_exp_f32 v177, v177\n" \
  "v_exp_f32 v178, v178\n" \
  "v_exp_f32 v179, v179\n" \
  "v_smfmac_f32_32x32x32_bf16 %[o2], v[198:201], v[160:167], %[idx]\n" \
  "v_exp_f32 v180, v180\n" \
  "v_exp_f32 v181, v181\n" \
  "v_exp_f32 v182, v182\n" \
  "v_exp_f32 v183, v183\n" \
  "v_mfma_f32_32x32x16_bf16 v[144:159], v[192:195], %[q1], 0\n" \
  "v_exp_f32 v168, v168\n" \
  "v_exp_f32 v169, v169\n" \
  "v_exp_f32 v170, v170\n" \
  "v_exp_f32 v171, v171\n" \
  "v_smfmac_f32_32x32x32_bf16 %[o3], v[198:201], v[176:183], %[idx]\n" \
  "ds_read_b128 v[198:201], %[av] offset:2240\n" \
  "v_exp_f32 v172, v172\n" \
  "v_exp_f32 v173, v173\n" \
  "v_exp_f32 v174, v174\n" \
  "v_exp_f32 v175, v175\n" \
  "v_mfma_f32_32x32x8_bf16 v[144:159], v[196:197], %[r1], v[144:159]\n" \
  "v_exp_f32 v184, v184\n" \
  "v_exp_f32 v185, v185\n" \
  "v_exp_f32 v186, v186\n" \
  "v_exp_f32 v187, v187\n" \
  "v_smfmac_f32_32x32x32_bf16 %[o2], v[202:205], v[168:175], %[idx]\n" \
  "v_exp_f32 v188, v188\n" \
  "v_exp_f32 v189, v189\n" \
  "v_exp_f32 v190, v190\n" \
  "v_exp_f32 v191, v191\n" \
  "s_setprio 1\n" \
  "v_mfma_f32_32x32x16_bf16 v[160:175], v[192:195], %[q2], 0\n" \
  "v_exp_f32 v128, v128\n" \
  "v_exp_f32 v129, v129\n" \
  "v_exp_f32 v130, v130\n" \
  "v_exp_f32 v131, v131\n" \
  "v_smfmac_f32_32x32x32_bf16 %[o3], v[202:205], v[184:191], %[idx]\n" \
  "ds_read_b128 v[202:205], %[av] offset:4480\n" \
  "v_exp_f32 v132, v132\n" \
  "v_exp_f32 v133, v133\n" \
  "v_exp_f32 v134, v134\n" \
  "v_exp_f32 v135, v135\n" \
  "v_mfma_f32_32x32x8_bf16 v[160:175], v[196:197], %[r2], v[160:175]\n" \
  "v_exp_f32 v144, v144\n" \
  "v_exp_f32 v145, v145\n" \
  "v_exp_f32 v146, v146\n" \
  "v_exp_f32 v147, v147\n" \
  "s_waitcnt lgkmcnt(1)\n" \
  "v_smfmac_f32_32x32x32_bf16 %[o0], v[198:201], v[128:135], %[idx]\n" \
  "v_exp_f32 v148, v148\n" \
  "v_exp_f32 v149, v149\n" \
  "v_exp_f32 v150, v150\n" \
  "v_exp_f32 v151, v151\n" \
  "v_mfma_f32_32x32x16_bf16 v[176:191], v[192:195], %[q3], 0\n" \
  "ds_read_b128 v[192:195], %[ak1] offset:2944\n" \
  "v_exp_f32 v136, v136\n" \
  "v_exp_f32 v137, v137\n" \
  "v_exp_f32 v138, v138\n" \
  "v_exp_f32 v139, v139\n" \
  "v_smfmac_f32_32x32x32_bf16 %[o1], v[198:201], v[144:151], %[idx]\n" \
  "v_exp_f32 v140, v140\n" \
  "v_exp_f32 v141, v141\n" \
  "v_exp_f32 v142, v142\n" \
  "v_exp_f32 v143, v143\n" \
  "v_mfma_f32_32x32x8_bf16 v[176:191], v[196:197], %[r3], v[176:191]\n" \
  "s_mov_b32 exec_hi, 0x3ffff\n" \
  "s_mov_b32 m0, s64\n" \
  "s_add_u32 s64, s64, 6400\n" \
  "global_load_lds_dwordx4 %[wv], s[62:63]\n" \
  "s_add_u32 s62, s62, 51200\n" \
  "s_addc_u32 s63, s63, 0\n" \
  "s_mov_b32 m0, s64\n" \
  "s_add_u32 s64, s64, 6400\n" \
  "global_load_lds_dwordx4 %[wv], s[62:63]\n" \
  "s_add_u32 s62, s62, 51200\n" \
  "s_addc_u32 s63, s63, 0\n" \
  "s_mov_b32 exec_hi, -1\n" \
  "ds_read_b64 v[196:197], %[ak2] offset:3968\n" \
  "v_exp_f32 v152, v152\n" \
  "v_exp_f32 v153, v153\n" \
  "v_exp_f32 v154, v154\n" \
  "v_exp_f32 v155, v155\n" \
  "s_waitcnt lgkmcnt(1)\n" \
  "v_smfmac_f32_32x32x32_bf16 %[o0], v[202:205], v[136:143], %[idx]\n" \
  "v_exp_f32 v156, v156\n" \
  "v_exp_f32 v157, v157\n" \
  "v_exp_f32 v158, v158\n" \
  "v_exp_f32 v159, v159\n" \
  "v_mfma_f32_32x32x16_bf16 v[128:143], v[192:195], %[q0], 0\n" \
  "v_exp_f32 v160, v160\n" \
  "v_exp_f32 v161, v161\n" \
  "v_exp_f32 v162, v162\n" \
  "v_exp_f32 v163, v163\n" \
  "v_smfmac_f32_32x32x32_bf16 %[o1], v[202:205], v[152:159], %[idx]\n" \
  "v_exp_f32 v164, v164\n" \
  "v_exp_f32 v165, v165\n" \
  "v_exp_f32 v166, v166\n" \
  "v_exp_f32 v167, v167\n" \
  "s_waitcnt lgkmcnt(0)\n" \
  "v_mfma_f32_32x32x8_bf16 v[128:143], v[196:197], %[r0], v[128:143]\n" \
  "s_setprio 0\n" \
  "v_exp_f32 v176, v176\n" \
  "v_exp_f32 v177, v177\n" \
  "v_exp_f32 v178, v178\n" \
  "v_exp_f32 v179, v179\n" \
  "v_smfmac_f32_32x32x32_bf16 %[o2], v[198:201], v[160:167], %[idx]\n" \
  "v_exp_f32 v180, v180\n" \
  "v_exp_f32 v181, v181\n" \
  "v_exp_f32 v182, v182\n" \
  "v_exp_f32 v183, v183\n" \
  "v_mfma_f32_32x32x16_bf16 v[144:159], v[192:195], %[q1], 0\n" \
  "v_exp_f32 v168, v168\n" \
  "v_exp_f32 v169, v169\n" \
  "v_exp_f32 v170, v170\n" \
  "v_exp_f32 v171, v171\n" \
  "v_smfmac_f32_32x32x32_bf16 %[o3], v[198:201], v[176:183], %[idx]\n" \
  "ds_read_b128 v[198:201], %[av] offset:5184\n" \
  "v_exp_f32 v172, v172\n" \
  "v_exp_f32 v173, v173\n" \
  "v_exp_f32 v174, v174\n" \
  "v_exp_f32 v175, v175\n" \
  "v_mfma_f32_32x32x8_bf16 v[144:159], v[196:197], %[r1], v[144:159]\n" \
  "v_exp_f32 v184, v184\n" \
  "v_exp_f32 v185, v185\n" \
  "v_exp_f32 v186, v186\n" \
  "v_exp_f32 v187, v187\n" \
  "v_smfmac_f32_32x32x32_bf16 %[o2], v[202:205], v[168:175], %[idx]\n" \
  "v_exp_f32 v188, v188\n" \
  "v_exp_f32 v189, v189\n" \
  "v_exp_f32 v190, v190\n" \
  "v_exp_f32 v191, v191\n" \
  "s_setprio 1\n" \
  "v_mfma_f32_32x32x16_bf16 v[160:175], v[192:195], %[q2], 0\n" \
  "v_exp_f32 v128, v128\n" \
  "v_exp_f32 v129, v129\n" \
  "v_exp_f32 v130, v130\n" \
  "v_exp_f32 v131, v131\n" \
  "v_smfmac_f32_32x32x32_bf16 %[o3], v[202:205], v[184:191], %[idx]\n" \
  "ds_read_b128 v[202:205], %[av] offset:7424\n" \
  "v_exp_f32 v132, v132\n" \
  "v_exp_f32 v133, v133\n" \
  "v_exp_f32 v134, v134\n" \
  "v_exp_f32 v135, v135\n" \
  "v_mfma_f32_32x32x8_bf16 v[160:175], v[196:197], %[r2], v[160:175]\n" \
  "v_exp_f32 v144, v144\n" \
  "v_exp_f32 v145, v145\n" \
  "v_exp_f32 v146, v146\n" \
  "v_exp_f32 v147, v147\n" \
  "s_waitcnt lgkmcnt(1)\n" \
  "v_smfmac_f32_32x32x32_bf16 %[o0], v[198:201], v[128:135], %[idx]\n" \
  "v_exp_f32 v148, v148\n" \
  "v_exp_f32 v149, v149\n" \
  "v_exp_f32 v150, v150\n" \
  "v_exp_f32 v151, v151\n" \
  "v_mfma_f32_32x32x16_bf16 v[176:191], v[192:195], %[q3], 0\n" \
  "ds_read_b128 v[192:195], %[ak1] offset:5888\n" \
  "v_exp_f32 v136, v136\n" \
  "v_exp_f32 v137, v137\n" \
  "v_exp_f32 v138, v138\n" \
  "v_exp_f32 v139, v139\n" \
  "v_smfmac_f32_32x32x32_bf16 %[o1], v[198:201], v[144:151], %[idx]\n" \
  "v_exp_f32 v140, v140\n" \
  "v_exp_f32 v141, v141\n" \
  "v_exp_f32 v142, v142\n" \
  "v_exp_f32 v143, v143\n" \
  "v_mfma_f32_32x32x8_bf16 v[176:191], v[196:197], %[r3], v[176:191]\n" \
  "ds_read_b64 v[196:197], %[ak2] offset:6912\n" \
  "v_exp_f32 v152, v152\n" \
  "v_exp_f32 v153, v153\n" \
  "v_exp_f32 v154, v154\n" \
  "v_exp_f32 v155, v155\n" \
  "s_waitcnt lgkmcnt(1)\n" \
  "v_smfmac_f32_32x32x32_bf16 %[o0], v[202:205], v[136:143], %[idx]\n" \
  "v_exp_f32 v156, v156\n" \
  "v_exp_f32 v157, v157\n" \
  "v_exp_f32 v158, v158\n" \
  "v_exp_f32 v159, v159\n" \
  "v_mfma_f32_32x32x16_bf16 v[128:143], v[192:195], %[q0], 0\n" \
  "v_exp_f32 v160, v160\n" \
  "v_exp_f32 v161, v161\n" \
  "v_exp_f32 v162, v162\n" \
  "v_exp_f32 v163, v163\n" \
  "v_smfmac_f32_32x32x32_bf16 %[o1], v[202:205], v[152:159], %[idx]\n" \
  "v_exp_f32 v164, v164\n" \
  "v_exp_f32 v165, v165\n" \
  "v_exp_f32 v166, v166\n" \
  "v_exp_f32 v167, v167\n" \
  "s_waitcnt lgkmcnt(0)\n" \
  "v_mfma_f32_32x32x8_bf16 v[128:143], v[196:197], %[r0], v[128:143]\n" \
  "s_setprio 0\n" \
  "v_exp_f32 v176, v176\n" \
  "v_exp_f32 v177, v177\n" \
  "v_exp_f32 v178, v178\n" \
  "v_exp_f32 v179, v179\n" \
  "v_smfmac_f32_32x32x32_bf16 %[o2], v[198:201], v[160:167], %[idx]\n" \
  "v_exp_f32 v180, v180\n" \
  "v_exp_f32 v181, v181\n" \
  "v_exp_f32 v182, v182\n" \
  "v_exp_f32 v183, v183\n" \
  "v_mfma_f32_32x32x16_bf16 v[144:159], v[192:195], %[q1], 0\n" \
  "v_exp_f32 v168, v168\n" \
  "v_exp_f32 v169, v169\n" \
  "v_exp_f32 v170, v170\n" \
  "v_exp_f32 v171, v171\n" \
  "v_smfmac_f32_32x32x32_bf16 %[o3], v[198:201], v[176:183], %[idx]\n" \
  "ds_read_b128 v[198:201], %[av] offset:8128\n" \
  "v_exp_f32 v172, v172\n" \
  "v_exp_f32 v173, v173\n" \
  "v_exp_f32 v174, v174\n" \
  "v_exp_f32 v175, v175\n" \
  "v_mfma_f32_32x32x8_bf16 v[144:159], v[196:197], %[r1], v[144:159]\n" \
  "v_exp_f32 v184, v184\n" \
  "v_exp_f32 v185, v185\n" \
  "v_exp_f32 v186, v186\n" \
  "v_exp_f32 v187, v187\n" \
  "v_smfmac_f32_32x32x32_bf16 %[o2], v[202:205], v[168:175], %[idx]\n" \
  "v_exp_f32 v188, v188\n" \
  "v_exp_f32 v189, v189\n" \
  "v_exp_f32 v190, v190\n" \
  "v_exp_f32 v191, v191\n" \
  "s_setprio 1\n" \
  "v_mfma_f32_32x32x16_bf16 v[160:175], v[192:195], %[q2], 0\n" \
  "v_exp_f32 v128, v128\n" \
  "v_exp_f32 v129, v129\n" \
  "v_exp_f32 v130, v130\n" \
  "v_exp_f32 v131, v131\n" \
  "v_smfmac_f32_32x32x32_bf16 %[o3], v[202:205], v[184:191], %[idx]\n" \
  "ds_read_b128 v[202:205], %[av] offset:10368\n" \
  "v_exp_f32 v132, v132\n" \
  "v_exp_f32 v133, v133\n" \
  "v_exp_f32 v134, v134\n" \
  "v_exp_f32 v135, v135\n" \
  "v_mfma_f32_32x32x8_bf16 v[160:175], v[196:197], %[r2], v[160:175]\n" \
  "v_exp_f32 v144, v144\n" \
  "v_exp_f32 v145, v145\n" \
  "v_exp_f32 v146, v146\n" \
  "v_exp_f32 v147, v147\n" \
  "s_waitcnt lgkmcnt(1)\n" \
  "v_smfmac_f32_32x32x32_bf16 %[o0], v[198:201], v[128:135], %[idx]\n" \
  "v_exp_f32 v148, v148\n" \
  "v_exp_f32 v149, v149\n" \
  "v_exp_f32 v150, v150\n" \
  "v_exp_f32 v151, v151\n" \
  "v_mfma_f32_32x32x16_bf16 v[176:191], v[192:195], %[q3], 0\n" \
  "ds_read_b128 v[192:195], %[ak1] offset:8832\n" \
  "v_exp_f32 v136, v136\n" \
  "v_exp_f32 v137, v137\n" \
  "v_exp_f32 v138, v138\n" \
  "v_exp_f32 v139, v139\n" \
  "v_smfmac_f32_32x32x32_bf16 %[o1], v[198:201], v[144:151], %[idx]\n" \
  "v_exp_f32 v140, v140\n" \
  "v_exp_f32 v141, v141\n" \
  "v_exp_f32 v142, v142\n" \
  "v_exp_f32 v143, v143\n" \
  "v_mfma_f32_32x32x8_bf16 v[176:191], v[196:197], %[r3], v[176:191]\n" \
  "ds_read_b64 v[196:197], %[ak2] offset:9856\n" \
  "v_exp_f32 v152, v152\n" \
  "v_exp_f32 v153, v153\n" \
  "v_exp_f32 v154, v154\n" \
  "v_exp_f32 v155, v155\n" \
  "s_waitcnt lgkmcnt(1)\n" \
  "v_smfmac_f32_32x32x32_bf16 %[o0], v[202:205], v[136:143], %[idx]\n" \
  "v_exp_f32 v156, v156\n" \
  "v_exp_f32 v157, v157\n" \
  "v_exp_f32 v158, v158\n" \
  "v_exp_f32 v159, v159\n" \
  "v_mfma_f32_32x32x16_bf16 v[128:143], v[192:195], %[q0], 0\n" \
  "v_exp_f32 v160, v160\n" \
  "v_exp_f32 v161, v161\n" \
  "v_exp_f32 v162, v162\n" \
  "v_exp_f32 v163, v163\n" \
  "v_smfmac_f32_32x32x32_bf16 %[o1], v[202:205], v[152:159], %[idx]\n" \
  "v_exp_f32 v164, v164\n" \
  "v_exp_f32 v165, v165\n" \
  "v_exp_f32 v166, v166\n" \
  "v_exp_f32 v167, v167\n" \
  "s_waitcnt lgkmcnt(0)\n" \
  "v_mfma_f32_32x32x8_bf16 v[128:143], v[196:197], %[r0], v[128:143]\n" \
  "s_setprio 0\n" \
  "v_exp_f32 v176, v176\n" \
  "v_exp_f32 v177, v177\n" \
  "v_exp_f32 v178, v178\n" \
  "v_exp_f32 v179, v179\n" \
  "v_smfmac_f32_32x32x32_bf16 %[o2], v[198:201], v[160:167], %[idx]\n" \
  "v_exp_f32 v180, v180\n" \
  "v_exp_f32 v181, v181\n" \
  "v_exp_f32 v182, v182\n" \
  "v_exp_f32 v183, v183\n" \
  "v_mfma_f32_32x32x16_bf16 v[144:159], v[192:195], %[q1], 0\n" \
  "v_exp_f32 v168, v168\n" \
  "v_exp_f32 v169, v169\n" \
  "v_exp_f32 v170, v170\n" \
  "v_exp_f32 v171, v171\n" \
  "v_smfmac_f32_32x32x32_bf16 %[o3], v[198:201], v[176:183], %[idx]\n" \
  "ds_read_b128 v[198:201], %[av] offset:11072\n" \
  "v_exp_f32 v172, v172\n" \
  "v_exp_f32 v173, v173\n" \
  "v_exp_f32 v174, v174\n" \
  "v_exp_f32 v175, v175\n" \
  "v_mfma_f32_32x32x8_bf16 v[144:159], v[196:197], %[r1], v[144:159]\n" \
  "v_exp_f32 v184, v184\n" \
  "v_exp_f32 v185, v185\n" \
  "v_exp_f32 v186, v186\n" \
  "v_exp_f32 v187, v187\n" \
  "v_smfmac_f32_32x32x32_bf16 %[o2], v[202:205], v[168:175], %[idx]\n" \
  "v_exp_f32 v188, v188\n" \
  "v_exp_f32 v189, v189\n" \
  "v_exp_f32 v190, v190\n" \
  "v_exp_f32 v191, v191\n" \
  "s_waitcnt vmcnt(0) lgkmcnt(0)\n" \
  "s_barrier\n" \
  "s_cmp_eq_u32 %[cnt], 1\n" \
  "s_cbranch_scc1 .Lskip%=\n" \
  "s_mov_b32 m0, %[m0a]\n" \
  "s_nop 0\n" \
  "global_load_lds_dwordx4 %[d0], s[60:61] sc1\n" \
  "global_load_lds_dwordx4 %[d1], s[60:61] offset:1024 sc1\n" \
  "global_load_lds_dwordx4 %[d2], s[60:61] offset:2048 sc1\n" \
  "s_add_u32 s60, s60, 11776\n" \
  "s_addc_u32 s61, s61, 0\n" \
  ".Lskip%=:\n" \
  "s_setprio 1\n" \
  "v_mfma_f32_32x32x16_bf16 v[160:175], v[192:195], %[q2], 0\n" \
  "v_exp_f32 v128, v128\n" \
  "v_exp_f32 v129, v129\n" \
  "v_exp_f32 v130, v130\n" \
  "v_exp_f32 v131, v131\n" \
  "v_smfmac_f32_32x32x32_bf16 %[o3], v[202:205], v[184:191], %[idx]\n" \
  "ds_read_b128 v[202:205], %[av] offset:25088\n" \
  "v_exp_f32 v132, v132\n" \
  "v_exp_f32 v133, v133\n" \
  "v_exp_f32 v134, v134\n" \
  "v_exp_f32 v135, v135\n" \
  "v_mfma_f32_32x32x8_bf16 v[160:175], v[196:197], %[r2], v[160:175]\n" \
  "v_exp_f32 v144, v144\n" \
  "v_exp_f32 v145, v145\n" \
  "v_exp_f32 v146, v146\n" \
  "v_exp_f32 v147, v147\n" \
  "s_waitcnt lgkmcnt(1)\n" \
  "v_smfmac_f32_32x32x32_bf16 %[o0], v[198:201], v[128:135], %[idx]\n" \
  "v_exp_f32 v148, v148\n" \
  "v_exp_f32 v149, v149\n" \
  "v_exp_f32 v150, v150\n" \
  "v_exp_f32 v151, v151\n" \
  "v_mfma_f32_32x32x16_bf16 v[176:191], v[192:195], %[q3], 0\n" \
  "ds_read_b128 v[192:195], %[ak1] offset:23552\n" \
  "v_exp_f32 v136, v136\n" \
  "v_exp_f32 v137, v137\n" \
  "v_exp_f32 v138, v138\n" \
  "v_exp_f32 v139, v139\n" \
  "v_smfmac_f32_32x32x32_bf16 %[o1], v[198:201], v[144:151], %[idx]\n" \
  "v_exp_f32 v140, v140\n" \
  "v_exp_f32 v141, v141\n" \
  "v_exp_f32 v142, v142\n" \
  "v_exp_f32 v143, v143\n" \
  "v_mfma_f32_32x32x8_bf16 v[176:191], v[196:197], %[r3], v[176:191]\n" \
  "ds_read_b64 v[196:197], %[ak2] offset:24576\n" \
  "v_exp_f32 v152, v152\n" \
  "v_exp_f32 v153, v153\n" \
  "v_exp_f32 v154, v154\n" \
  "v_exp_f32 v155, v155\n" \
  "s_waitcnt lgkmcnt(1)\n" \
  "v_smfmac_f32_32x32x32_bf16 %[o0], v[202:205], v[136:143], %[idx]\n" \
  "v_exp_f32 v156, v156\n" \
  "v_exp_f32 v157, v157\n" \
  "v_exp_f32 v158, v158\n" \
  "v_exp_f32 v159, v159\n" \
  "v_mfma_f32_32x32x16_bf16 v[128:143], v[192:195], %[q0], 0\n" \
  "v_exp_f32 v160, v160\n" \
  "v_exp_f32 v161, v161\n" \
  "v_exp_f32 v162, v162\n" \
  "v_exp_f32 v163, v163\n" \
  "v_smfmac_f32_32x32x32_bf16 %[o1], v[202:205], v[152:159], %[idx]\n" \
  "v_exp_f32 v164, v164\n" \
  "v_exp_f32 v165, v165\n" \
  "v_exp_f32 v166, v166\n" \
  "v_exp_f32 v167, v167\n" \
  "s_waitcnt lgkmcnt(0)\n" \
  "v_mfma_f32_32x32x8_bf16 v[128:143], v[196:197], %[r0], v[128:143]\n" \
  "s_setprio 0\n" \
  "v_exp_f32 v176, v176\n" \
  "v_exp_f32 v177, v177\n" \
  "v_exp_f32 v178, v178\n" \
  "v_exp_f32 v179, v179\n" \
  "v_smfmac_f32_32x32x32_bf16 %[o2], v[198:201], v[160:167], %[idx]\n" \
  "v_exp_f32 v180, v180\n" \
  "v_exp_f32 v181, v181\n" \
  "v_exp_f32 v182, v182\n" \
  "v_exp_f32 v183, v183\n" \
  "v_mfma_f32_32x32x16_bf16 v[144:159], v[192:195], %[q1], 0\n" \
  "v_exp_f32 v168, v168\n" \
  "v_exp_f32 v169, v169\n" \
  "v_exp_f32 v170, v170\n" \
  "v_exp_f32 v171, v171\n" \
  "v_smfmac_f32_32x32x32_bf16 %[o3], v[198:201], v[176:183], %[idx]\n" \
  "ds_read_b128 v[198:201], %[av] offset:25792\n" \
  "v_exp_f32 v172, v172\n" \
  "v_exp_f32 v173, v173\n" \
  "v_exp_f32 v174, v174\n" \
  "v_exp_f32 v175, v175\n" \
  "v_mfma_f32_32x32x8_bf16 v[144:159], v[196:197], %[r1], v[144:159]\n" \
  "v_exp_f32 v184, v184\n" \
  "v_exp_f32 v185, v185\n" \
  "v_exp_f32 v186, v186\n" \
  "v_exp_f32 v187, v187\n" \
  "v_smfmac_f32_32x32x32_bf16 %[o2], v[202:205], v[168:175], %[idx]\n" \
  "v_exp_f32 v188, v188\n" \
  "v_exp_f32 v189, v189\n" \
  "v_exp_f32 v190, v190\n" \
  "v_exp_f32 v191, v191\n" \
  "s_setprio 1\n" \
  "v_mfma_f32_32x32x16_bf16 v[160:175], v[192:195], %[q2], 0\n" \
  "v_exp_f32 v128, v128\n" \
  "v_exp_f32 v129, v129\n" \
  "v_exp_f32 v130, v130\n" \
  "v_exp_f32 v131, v131\n" \
  "v_smfmac_f32_32x32x32_bf16 %[o3], v[202:205], v[184:191], %[idx]\n" \
  "ds_read_b128 v[202:205], %[av] offset:28032\n" \
  "v_exp_f32 v132, v132\n" \
  "v_exp_f32 v133, v133\n" \
  "v_exp_f32 v134, v134\n" \
  "v_exp_f32 v135, v135\n" \
  "v_mfma_f32_32x32x8_bf16 v[160:175], v[196:197], %[r2], v[160:175]\n" \
  "v_exp_f32 v144, v144\n" \
  "v_exp_f32 v145, v145\n" \
  "v_exp_f32 v146, v146\n" \
  "v_exp_f32 v147, v147\n" \
  "s_waitcnt lgkmcnt(1)\n" \
  "v_smfmac_f32_32x32x32_bf16 %[o0], v[198:201], v[128:135], %[idx]\n" \
  "v_exp_f32 v148, v148\n" \
  "v_exp_f32 v149, v149\n" \
  "v_exp_f32 v150, v150\n" \
  "v_exp_f32 v151, v151\n" \
  "v_mfma_f32_32x32x16_bf16 v[176:191], v[192:195], %[q3], 0\n" \
  "ds_read_b128 v[192:195], %[ak1] offset:26496\n" \
  "v_exp_f32 v136, v136\n" \
  "v_exp_f32 v137, v137\n" \
  "v_exp_f32 v138, v138\n" \
  "v_exp_f32 v139, v139\n" \
  "v_smfmac_f32_32x32x32_bf16 %[o1], v[198:201], v[144:151], %[idx]\n" \
  "v_exp_f32 v140, v140\n" \
  "v_exp_f32 v141, v141\n" \
  "v_exp_f32 v142, v142\n" \
  "v_exp_f32 v143, v143\n" \
  "v_mfma_f32_32x32x8_bf16 v[176:191], v[196:197], %[r3], v[176:191]\n" \
  "s_mov_b32 exec_hi, 0x3ffff\n" \
  "s_mov_b32 m0, s64\n" \
  "s_add_u32 s64, s64, 6400\n" \
  "global_load_lds_dwordx4 %[wv], s[62:63]\n" \
  "s_add_u32 s62, s62, 51200\n" \
  "s_addc_u32 s63, s63, 0\n" \
  "s_mov_b32 m0, s64\n" \
  "s_add_u32 s64, s64, 6400\n" \
  "global_load_lds_dwordx4 %[wv], s[62:63]\n" \
  "s_add_u32 s62, s62, 51200\n" \
  "s_addc_u32 s63, s63, 0\n" \
  "s_mov_b32 exec_hi, -1\n" \
  "ds_read_b64 v[196:197], %[ak2] offset:27520\n" \
  "v_exp_f32 v152, v152\n" \
  "v_exp_f32 v153, v153\n" \
  "v_exp_f32 v154, v154\n" \
  "v_exp_f32 v155, v155\n" \
  "s_waitcnt lgkmcnt(1)\n" \
  "v_smfmac_f32_32x32x32_bf16 %[o0], v[202:205], v[136:143], %[idx]\n" \
  "v_exp_f32 v156, v156\n" \
  "v_exp_f32 v157, v157\n" \
  "v_exp_f32 v158, v158\n" \
  "v_exp_f32 v159, v159\n" \
  "v_mfma_f32_32x32x16_bf16 v[128:143], v[192:195], %[q0], 0\n" \
  "v_exp_f32 v160, v160\n" \
  "v_exp_f32 v161, v161\n" \
  "v_exp_f32 v162, v162\n" \
  "v_exp_f32 v163, v163\n" \
  "v_smfmac_f32_32x32x32_bf16 %[o1], v[202:205], v[152:159], %[idx]\n" \
  "v_exp_f32 v164, v164\n" \
  "v_exp_f32 v165, v165\n" \
  "v_exp_f32 v166, v166\n" \
  "v_exp_f32 v167, v167\n" \
  "s_waitcnt lgkmcnt(0)\n" \
  "v_mfma_f32_32x32x8_bf16 v[128:143], v[196:197], %[r0], v[128:143]\n" \
  "s_setprio 0\n" \
  "v_exp_f32 v176, v176\n" \
  "v_exp_f32 v177, v177\n" \
  "v_exp_f32 v178, v178\n" \
  "v_exp_f32 v179, v179\n" \
  "v_smfmac_f32_32x32x32_bf16 %[o2], v[198:201], v[160:167], %[idx]\n" \
  "v_exp_f32 v180, v180\n" \
  "v_exp_f32 v181, v181\n" \
  "v_exp_f32 v182, v182\n" \
  "v_exp_f32 v183, v183\n" \
  "v_mfma_f32_32x32x16_bf16 v[144:159], v[192:195], %[q1], 0\n" \
  "v_exp_f32 v168, v168\n" \
  "v_exp_f32 v169, v169\n" \
  "v_exp_f32 v170, v170\n" \
  "v_exp_f32 v171, v171\n" \
  "v_smfmac_f32_32x32x32_bf16 %[o3], v[198:201], v[176:183], %[idx]\n" \
  "ds_read_b128 v[198:201], %[av] offset:28736\n" \
  "v_exp_f32 v172, v172\n" \
  "v_exp_f32 v173, v173\n" \
  "v_exp_f32 v174, v174\n" \
  "v_exp_f32 v175, v175\n" \
  "v_mfma_f32_32x32x8_bf16 v[144:159], v[196:197], %[r1], v[144:159]\n" \
  "v_exp_f32 v184, v184\n" \
  "v_exp_f32 v185, v185\n" \
  "v_exp_f32 v186, v186\n" \
  "v_exp_f32 v187, v187\n" \
  "v_smfmac_f32_32x32x32_bf16 %[o2], v[202:205], v[168:175], %[idx]\n" \
  "v_exp_f32 v188, v188\n" \
  "v_exp_f32 v189, v189\n" \
  "v_exp_f32 v190, v190\n" \
  "v_exp_f32 v191, v191\n" \
  "s_setprio 1\n" \
  "v_mfma_f32_32x32x16_bf16 v[160:175], v[192:195], %[q2], 0\n" \
  "v_exp_f32 v128, v128\n" \
  "v_exp_f32 v129, v129\n" \
  "v_exp_f32 v130, v130\n" \
  "v_exp_f32 v131, v131\n" \
  "v_smfmac_f32_32x32x32_bf16 %[o3], v[202:205], v[184:191], %[idx]\n" \
  "ds_read_b128 v[202:205], %[av] offset:30976\n" \
  "v_exp_f32 v132, v132\n" \
  "v_exp_f32 v133, v133\n" \
  "v_exp_f32 v134, v134\n" \
  "v_exp_f32 v135, v135\n" \
  "v_mfma_f32_32x32x8_bf16 v[160:175], v[196:197], %[r2], v[160:175]\n" \
  "v_exp_f32 v144, v144\n" \
  "v_exp_f32 v145, v145\n" \
  "v_exp_f32 v146, v146\n" \
  "v_exp_f32 v147, v147\n" \
  "s_waitcnt lgkmcnt(1)\n" \
  "v_smfmac_f32_32x32x32_bf16 %[o0], v[198:201], v[128:135], %[idx]\n" \
  "v_exp_f32 v148, v148\n" \
  "v_exp_f32 v149, v149\n" \
  "v_exp_f32 v150, v150\n" \
  "v_exp_f32 v151, v151\n" \
  "v_mfma_f32_32x32x16_bf16 v[176:191], v[192:195], %[q3], 0\n" \
  "ds_read_b128 v[192:195], %[ak1] offset:29440\n" \
  "v_exp_f32 v136, v136\n" \
  "v_exp_f32 v137, v137\n" \
  "v_exp_f32 v138, v138\n" \
  "v_exp_f32 v139, v139\n" \
  "v_smfmac_f32_32x32x32_bf16 %[o1], v[198:201], v[144:151], %[idx]\n" \
  "v_exp_f32 v140, v140\n" \
  "v_exp_f32 v141, v141\n" \
  "v_exp_f32 v142, v142\n" \
  "v_exp_f32 v143, v143\n" \
  "v_mfma_f32_32x32x8_bf16 v[176:191], v[196:197], %[r3], v[176:191]\n" \
  "ds_read_b64 v[196:197], %[ak2] offset:30464\n" \
  "v_exp_f32 v152, v152\n" \
  "v_exp_f32 v153, v153\n" \
  "v_exp_f32 v154, v154\n" \
  "v_exp_f32 v155, v155\n" \
  "s_waitcnt lgkmcnt(1)\n" \
  "v_smfmac_f32_32x32x32_bf16 %[o0], v[202:205], v[136:143], %[idx]\n" \
  "v_exp_f32 v156, v156\n" \
  "v_exp_f32 v157, v157\n" \
  "v_exp_f32 v158, v158\n" \
  "v_exp_f32 v159, v159\n" \
  "v_mfma_f32_32x32x16_bf16 v[128:143], v[192:195], %[q0], 0\n" \
  "v_exp_f32 v160, v160\n" \
  "v_exp_f32 v161, v161\n" \
  "v_exp_f32 v162, v162\n" \
  "v_exp_f32 v163, v163\n" \
  "v_smfmac_f32_32x32x32_bf16 %[o1], v[202:205], v[152:159], %[idx]\n" \
  "v_exp_f32 v164, v164\n" \
  "v_exp_f32 v165, v165\n" \
  "v_exp_f32 v166, v166\n" \
  "v_exp_f32 v167, v167\n" \
  "s_waitcnt lgkmcnt(0)\n" \
  "v_mfma_f32_32x32x8_bf16 v[128:143], v[196:197], %[r0], v[128:143]\n" \
  "s_setprio 0\n" \
  "v_exp_f32 v176, v176\n" \
  "v_exp_f32 v177, v177\n" \
  "v_exp_f32 v178, v178\n" \
  "v_exp_f32 v179, v179\n" \
  "v_smfmac_f32_32x32x32_bf16 %[o2], v[198:201], v[160:167], %[idx]\n" \
  "v_exp_f32 v180, v180\n" \
  "v_exp_f32 v181, v181\n" \
  "v_exp_f32 v182, v182\n" \
  "v_exp_f32 v183, v183\n" \
  "v_mfma_f32_32x32x16_bf16 v[144:159], v[192:195], %[q1], 0\n" \
  "v_exp_f32 v168, v168\n" \
  "v_exp_f32 v169, v169\n" \
  "v_exp_f32 v170, v170\n" \
  "v_exp_f32 v171, v171\n" \
  "v_smfmac_f32_32x32x32_bf16 %[o3], v[198:201], v[176:183], %[idx]\n" \
  "ds_read_b128 v[198:201], %[av] offset:31680\n" \
  "v_exp_f32 v172, v172\n" \
  "v_exp_f32 v173, v173\n" \
  "v_exp_f32 v174, v174\n" \
  "v_exp_f32 v175, v175\n" \
  "v_mfma_f32_32x32x8_bf16 v[144:159], v[196:197], %[r1], v[144:159]\n" \
  "v_exp_f32 v184, v184\n" \
  "v_exp_f32 v185, v185\n" \
  "v_exp_f32 v186, v186\n" \
  "v_exp_f32 v187, v187\n" \
  "v_smfmac_f32_32x32x32_bf16 %[o2], v[202:205], v[168:175], %[idx]\n" \
  "v_exp_f32 v188, v188\n" \
  "v_exp_f32 v189, v189\n" \
  "v_exp_f32 v190, v190\n" \
  "v_exp_f32 v191, v191\n" \
  "s_setprio 1\n" \
  "v_mfma_f32_32x32x16_bf16 v[160:175], v[192:195], %[q2], 0\n" \
  "v_exp_f32 v128, v128\n" \
  "v_exp_f32 v129, v129\n" \
  "v_exp_f32 v130, v130\n" \
  "v_exp_f32 v131, v131\n" \
  "v_smfmac_f32_32x32x32_bf16 %[o3], v[202:205], v[184:191], %[idx]\n" \
  "ds_read_b128 v[202:205], %[av] offset:33920\n" \
  "v_exp_f32 v132, v132\n" \
  "v_exp_f32 v133, v133\n" \
  "v_exp_f32 v134, v134\n" \
  "v_exp_f32 v135, v135\n" \
  "v_mfma_f32_32x32x8_bf16 v[160:175], v[196:197], %[r2], v[160:175]\n" \
  "v_exp_f32 v144, v144\n" \
  "v_exp_f32 v145, v145\n" \
  "v_exp_f32 v146, v146\n" \
  "v_exp_f32 v147, v147\n" \
  "s_waitcnt lgkmcnt(1)\n" \
  "v_smfmac_f32_32x32x32_bf16 %[o0], v[198:201], v[128:135], %[idx]\n" \
  "v_exp_f32 v148, v148\n" \
  "v_exp_f32 v149, v149\n" \
  "v_exp_f32 v150, v150\n" \
  "v_exp_f32 v151, v151\n" \
  "v_mfma_f32_32x32x16_bf16 v[176:191], v[192:195], %[q3], 0\n" \
  "ds_read_b128 v[192:195], %[ak1] offset:32384\n" \
  "v_exp_f32 v136, v136\n" \
  "v_exp_f32 v137, v137\n" \
  "v_exp_f32 v138, v138\n" \
  "v_exp_f32 v139, v139\n" \
  "v_smfmac_f32_32x32x32_bf16 %[o1], v[198:201], v[144:151], %[idx]\n" \
  "v_exp_f32 v140, v140\n" \
  "v_exp_f32 v141, v141\n" \
  "v_exp_f32 v142, v142\n" \
  "v_exp_f32 v143, v143\n" \
  "v_mfma_f32_32x32x8_bf16 v[176:191], v[196:197], %[r3], v[176:191]\n" \
  "ds_read_b64 v[196:197], %[ak2] offset:33408\n" \
  "v_exp_f32 v152, v152\n" \
  "v_exp_f32 v153, v153\n" \
  "v_exp_f32 v154, v154\n" \
  "v_exp_f32 v155, v155\n" \
  "s_waitcnt lgkmcnt(1)\n" \
  "v_smfmac_f32_32x32x32_bf16 %[o0], v[202:205], v[136:143], %[idx]\n" \
  "v_exp_f32 v156, v156\n" \
  "v_exp_f32 v157, v157\n" \
  "v_exp_f32 v158, v158\n" \
  "v_exp_f32 v159, v159\n" \
  "v_mfma_f32_32x32x16_bf16 v[128:143], v[192:195], %[q0], 0\n" \
  "v_exp_f32 v160, v160\n" \
  "v_exp_f32 v161, v161\n" \
  "v_exp_f32 v162, v162\n" \
  "v_exp_f32 v163, v163\n" \
  "v_smfmac_f32_32x32x32_bf16 %[o1], v[202:205], v[152:159], %[idx]\n" \
  "v_exp_f32 v164, v164\n" \
  "v_exp_f32 v165, v165\n" \
  "v_exp_f32 v166, v166\n" \
  "v_exp_f32 v167, v167\n" \
  "s_waitcnt lgkmcnt(0)\n" \
  "v_mfma_f32_32x32x8_bf16 v[128:143], v[196:197], %[r0], v[128:143]\n" \
  "s_setprio 0\n" \
  "v_exp_f32 v176, v176\n" \
  "v_exp_f32 v177, v177\n" \
  "v_exp_f32 v178, v178\n" \
  "v_exp_f32 v179, v179\n" \
  "v_smfmac_f32_32x32x32_bf16 %[o2], v[198:201], v[160:167], %[idx]\n" \
  "v_exp_f32 v180, v180\n" \
  "v_exp_f32 v181, v181\n" \
  "v_exp_f32 v182, v182\n" \
  "v_exp_f32 v183, v183\n" \
  "v_mfma_f32_32x32x16_bf16 v[144:159], v[192:195], %[q1], 0\n" \
  "v_exp_f32 v168, v168\n" \
  "v_exp_f32 v169, v169\n" \
  "v_exp_f32 v170, v170\n" \
  "v_exp_f32 v171, v171\n" \
  "v_smfmac_f32_32x32x32_bf16 %[o3], v[198:201], v[176:183], %[idx]\n" \
  "ds_read_b128 v[198:201], %[av] offset:34624\n" \
  "v_exp_f32 v172, v172\n" \
  "v_exp_f32 v173, v173\n" \
  "v_exp_f32 v174, v174\n" \
  "v_exp_f32 v175, v175\n" \
  "v_mfma_f32_32x32x8_bf16 v[144:159], v[196:197], %[r1], v[144:159]\n" \
  "v_exp_f32 v184, v184\n" \
  "v_exp_f32 v185, v185\n" \
  "v_exp_f32 v186, v186\n" \
  "v_exp_f32 v187, v187\n" \
  "v_smfmac_f32_32x32x32_bf16 %[o2], v[202:205], v[168:175], %[idx]\n" \
  "v_exp_f32 v188, v188\n" \
  "v_exp_f32 v189, v189\n" \
  "v_exp_f32 v190, v190\n" \
  "v_exp_f32 v191, v191\n" \
  "s_sub_u32 %[cnt], %[cnt], 1\n" \
  "s_cmp_lg_u32 %[cnt], 0\n" \
  "s_cbranch_scc1 .Lloop%=\n" \
  "s_nop 1\n" \
  "v_smfmac_f32_32x32x32_bf16 %[o3], v[202:205], v[184:191], %[idx]\n" \
  "s_waitcnt lgkmcnt(0)\n" \
  "s_mov_b32 m0, %[sv]\n" \
  "s_setprio 0\n" \
  "s_nop 15\n" \

#define ATTN_CLOB "v128", "v129", "v130", "v131", "v132", "v133", "v134", "v135", "v136", "v137", "v138", "v139", "v140", "v141", "v142", "v143", "v144", "v145", "v146", "v147", "v148", "v149", "v150", "v151", "v152", "v153", "v154", "v155", "v156", "v157", "v158", "v159", "v160", "v161", "v162", "v163", "v164", "v165", "v166", "v167", "v168", "v169", "v170", "v171", "v172", "v173", "v174", "v175", "v176", "v177", "v178", "v179", "v180", "v181", "v182", "v183", "v184", "v185", "v186", "v187", "v188", "v189", "v190", "v191", "v192", "v193", "v194", "v195", "v196", "v197", "v198", "v199", "v200", "v201", "v202", "v203", "v204", "v205", "s60", "s61", "s62", "s63", "s64", "scc", "memory"


__device__ __forceinline__ unsigned cvtpk(float lo, float hi) { f32x2 v = {lo, hi}; bf16x2 b = __builtin_convertvector(v, bf16x2); return __builtin_bit_cast(unsigned, b); }
__device__ __forceinline__ u32x2 pack4(f32x4 v) { return u32x2{cvtpk(v[0], v[1]), cvtpk(v[2], v[3])}; }
__device__ __forceinline__ s16x4 vtr(lds_cptr p) { return __builtin_bit_cast(s16x4, __builtin_amdgcn_ds_read_tr16_b64_v4i16((__attribute__((address_space(3))) v4i16_t*)p)); }
__device__ __forceinline__ bf16x8 trfrag(lds_cptr p0, lds_cptr p1) {
  s16x4 a = vtr(p0), b = vtr(p1);
  s16x8 r = {a[0], a[1], a[2], a[3], b[0], b[1], b[2], b[3]};
  return __builtin_bit_cast(bf16x8, r);
}
__device__ __forceinline__ bf16x8 ldsfrag(lds_cptr p) { return *(const __attribute__((address_space(3))) bf16x8*)p; }
__device__ __forceinline__ const char* uni(const void* p) {
  const unsigned long long v = (unsigned long long)p;
  const unsigned lo = __builtin_amdgcn_readfirstlane((unsigned)v), hi = __builtin_amdgcn_readfirstlane((unsigned)(v >> 32));
  return (const char*)(((unsigned long long)hi << 32) | lo);
}
__device__ __forceinline__ void glds(const void* sbase, unsigned voff, unsigned lds_dst) {
  unsigned sv;
  asm volatile("s_mov_b32 %0, m0\n\ts_mov_b32 m0, %3\n\ts_nop 0\n\tglobal_load_lds_dwordx4 %1, %2\n\ts_mov_b32 m0, %0" : "=&s"(sv) : "v"(voff), "s"(uni(sbase)), "s"(lds_dst) : "memory");
}
template <int N> __device__ __forceinline__ void waitbar() { asm volatile("s_waitcnt vmcnt(%0) lgkmcnt(0)\n\ts_barrier" :: "n"(N) : "memory"); }
template <int N> __device__ __forceinline__ void waitvm() { asm volatile("s_waitcnt vmcnt(%0)" :: "n"(N) : "memory"); }
__device__ __forceinline__ void bar() { asm volatile("s_waitcnt lgkmcnt(0)\n\ts_barrier" ::: "memory"); }
__device__ __forceinline__ rsrc_t mkrsrc(const void* p, unsigned bytes) { return __builtin_amdgcn_make_buffer_rsrc((void*)p, 0, bytes, 0x00020000); }
__device__ __forceinline__ void st16(rsrc_t r, unsigned off, u32x4 v) { __builtin_amdgcn_raw_buffer_store_b128(v, r, off, 0, 16); }
__device__ __forceinline__ void st8(rsrc_t r, unsigned off, u32x2 v) { __builtin_amdgcn_raw_buffer_store_b64(v, r, off, 0, 16); }
__device__ __forceinline__ u32x4 ld16(rsrc_t r, unsigned off) { return __builtin_amdgcn_raw_buffer_load_b128(r, off, 0, 16); }
__device__ __forceinline__ u32x2 ld8(rsrc_t r, unsigned off) { return __builtin_amdgcn_raw_buffer_load_b64(r, off, 0, 16); }
__device__ __forceinline__ u32x4 pair16(u32x2 a, u32x2 b) {
  auto x = __builtin_amdgcn_permlane16_swap(a[0], b[0], false, false);
  auto y = __builtin_amdgcn_permlane16_swap(a[1], b[1], false, false);
  return u32x4{x[0], y[0], x[1], y[1]};
}
#define MFMA16(a, b, c) __builtin_amdgcn_mfma_f32_16x16x32_bf16(a, b, c, 0, 0, 0)
#define BF8(x) __builtin_bit_cast(bf16x8, x)

__device__ __forceinline__ void flag_set(char* ws, int idx, unsigned tag) {
  __hip_atomic_store((unsigned*)(ws + FL_OFF) + idx, tag, __ATOMIC_RELAXED, __HIP_MEMORY_SCOPE_AGENT);
}
__device__ __forceinline__ void flags_wait(char* ws, int idx, unsigned tag) {
  const unsigned* p = (const unsigned*)(ws + FL_OFF) + (idx < 0 ? 0 : idx);
  for (unsigned it = 0; it < SPIN_MAX; ++it) {
    const unsigned v = __hip_atomic_load(p, __ATOMIC_RELAXED, __HIP_MEMORY_SCOPE_AGENT);
    if (__all(idx < 0 || v == tag)) break;
    __builtin_amdgcn_s_sleep(2);
  }
  asm volatile("" ::: "memory");
}

constexpr int PJ_X = 0, PJ_W = 16384, PJ_STG = 36864, PJ_KV = 4 * PJ_STG, PJ_KVSTG = 2560, PROJ_LDS = 4 * PJ_STG + 3 * PJ_KVSTG;

template <int SEC, int NKV, int DUTY>
__device__ __forceinline__ void proj_producer(const char* x, const char* w, int ldw2, const char* wk, const char* wv, char* ws, int rb, int cg, char* lds, int pw, int lane) {
  const unsigned lds0 = (unsigned)(uintptr_t)lds;
  unsigned xo[4], wo[5];
#pragma unroll
  for (int i = 0; i < 4; ++i) { const int tok = 8 * (4 * pw + i) + (lane >> 3); xo[i] = tok * 1280 + (((lane & 7) ^ (tok & 7)) << 4); }
#pragma unroll
  for (int i = 0; i < 5; ++i) { const int pos = 1024 * (5 * pw + i) + 16 * lane, row = pos / 320, cp = (pos - row * 320) >> 4; wo[i] = row * ldw2 + ((cp ^ (((row >> 3) & 1) << 1)) << 4); }
  const char* kvp = uni(DUTY == 2 ? (const void*)wv : (const void*)wk);
  const char* xs = x + (size_t)rb * 128 * 1280;
  auto issue = [&](int c) {
    const unsigned dst = __builtin_amdgcn_readfirstlane(lds0 + (c & 3) * PJ_STG);
    const char* xb_ = xs + c * 128; const char* wb_ = w + (size_t)c * 64 * ldw2;
#pragma unroll
    for (int i = 0; i < 4; ++i) glds(xb_, xo[i], dst + PJ_X + (4 * pw + i) * 1024);
#pragma unroll
    for (int i = 0; i < 5; ++i) glds(wb_, wo[i], dst + PJ_W + (5 * pw + i) * 1024);
  };
  auto issue_kv = [&](int c) {
    if (NKV) {
      const unsigned kd = __builtin_amdgcn_readfirstlane(lds0 + PJ_KV + (c % 3) * PJ_KVSTG + pw * 1024);
      if (pw < 2 || lane < 32) glds(kvp + c * 2560 + pw * 1024, lane * 16, kd);
    }
  };
  const int g = lane >> 4, i16 = lane & 15, q = i16 >> 2, pp = i16 & 3;
  const int erow = (cg & 1) * 64 + 16 * pw + i16;
  const unsigned exa = PJ_X + erow * 128 + ((g ^ (erow & 7)) << 4), ew0 = PJ_KV + (8 * g + q) * 40 + 8 * pp, ew1 = PJ_KV + (8 * g + q) * 40 + 32;
  f32x4 e0 = {}, e1 = {};
  auto duty = [&](int c) {
    if (DUTY) {
      lds_cptr L = (lds_cptr)lds;
#pragma unroll
      for (int ks = 0; ks < 2; ++ks) {
        const bf16x8 xf = ldsfrag(L + (c & 3) * PJ_STG + (exa ^ (ks * 64)));
        lds_cptr kb = L + (c % 3) * PJ_KVSTG + ks * 1280;
        const bf16x8 w0 = trfrag(kb + ew0, kb + ew0 + 160), w1 = trfrag(kb + ew1, kb + ew1 + 160);
        e0 = DUTY == 2 ? MFMA16(xf, w0, e0) : MFMA16(w0, xf, e0);
        e1 = DUTY == 2 ? MFMA16(xf, w1, e1) : MFMA16(w1, xf, e1);
      }
    }
  };
  issue_kv(0); issue(0); issue_kv(1); issue(1);
  waitbar<9 + NKV>();
  issue_kv(2); issue(2); issue(3);
  duty(0);
#define PJ_PSTEP(K) do { waitbar<((K) + 2 <= 9 ? 9 + NKV : 0) + ((K) + 3 <= 9 ? 9 : 0)>(); if ((K) + 3 < 10) issue_kv((K) + 3); if ((K) + 4 < 10) issue((K) + 4); duty((K) + 1); } while (0)
  PJ_PSTEP(0); PJ_PSTEP(1); PJ_PSTEP(2); PJ_PSTEP(3); PJ_PSTEP(4); PJ_PSTEP(5); PJ_PSTEP(6); PJ_PSTEP(7); PJ_PSTEP(8);
  const rsrc_t R = mkrsrc(ws, WS_USED);
  if (SEC == 2) {
    const int i = pw * 64 + lane, head = cg * 8 + (i >> 5), tl = (i >> 3) & 3, s_ = (i >> 2) & 1, hA = (i >> 1) & 1, z = i & 1;
    const unsigned v = z ? 0u : 0x3F803F80u;
    st16(R, KV_OFF + head * STREAM + (rb * 4 + tl + s_) * REC + (s_ == 0 ? OFF_VS0 : OFF_VS1) + hA * 352 + 320 + z * 16, u32x4{v, v, v, v});
  }
  if (DUTY) {
    const unsigned st = KV_OFF + 32 * STREAM;
    const int tk = rb * 4 + 2 * (cg & 1) + (pw >> 1), eu = pw & 1, n = 16 * eu + i16;
    const u32x2 p0 = pack4(e0), p1 = pack4(e1);
    if (DUTY == 1) {
      st8(R, st + tk * REC + ((g >> 1) * 32 + n) * 16 + (g & 1) * 8, p0);
      if (g < 2) st8(R, st + tk * REC + OFF_K2 + g * 256 + n * 8, g == 0 ? p1 : u32x2{0x3F803F80u, 0u});
    } else {
      const unsigned rec = st + (tk + eu) * REC + (eu == 0 ? OFF_VS0 : OFF_VS1) + (g >> 1) * 352 + (g & 1) * 8;
      st8(R, rec + i16 * 16, p0);
      if (i16 < 6) st8(R, rec + (16 + i16) * 16, i16 < 4 ? p1 : (i16 == 4 ? u32x2{0x3F803F80u, 0x3F803F80u} : u32x2{0u, 0u}));
    }
  }
}

template <int SEC>
__device__ __forceinline__ void proj_consumer(char* ws, int rb, int cg, char* lds, int wid, int lane) {
  const int th = wid & 1, ch = wid >> 1;
  const int g = lane >> 4, i16 = lane & 15, q = i16 >> 2, pp = i16 & 3;
  unsigned xa_[4], wa_[5];
#pragma unroll
  for (int u = 0; u < 4; ++u) { const int row = th * 64 + 16 * u + i16; xa_[u] = PJ_X + row * 128 + ((g ^ (row & 7)) << 4); }
#pragma unroll
  for (int t = 0; t < 5; ++t) {
    int cb;
    if (SEC == 2) cb = (80 * ch + 16 * t + 4 * pp) * 2;
    else cb = t < 4 ? 40 * (4 * ch + t) + 8 * pp : 40 * (4 * ch + pp) + 32;
    wa_[t] = PJ_W + (8 * g + q) * 320 + (cb ^ (32 * (g & 1)));
  }
  f32x4 acc[4][5];
#pragma unroll
  for (int u = 0; u < 4; ++u)
#pragma unroll
    for (int t = 0; t < 5; ++t) acc[u][t] = f32x4{};
  bf16x8 xf[2][4], wf[2][5];
  lds_cptr L = (lds_cptr)lds;
#define PJ_LOAD(S, C, KS) do { lds_cptr sb_ = L + ((C) & 3) * PJ_STG; \
    _Pragma("unroll") for (int t = 0; t < 5; ++t) { wf[S][t] = trfrag(sb_ + wa_[t] + (KS) * 10240, sb_ + wa_[t] + (KS) * 10240 + 1280); if (t < 4) xf[S][t] = ldsfrag(sb_ + (xa_[t] ^ ((KS) * 64))); } } while (0)
#define PJ_MMA(S) do { \
    _Pragma("unroll") for (int t = 0; t < 5; ++t) _Pragma("unroll") for (int u = 0; u < 4; ++u) \
      acc[u][t] = SEC == 2 ? MFMA16(xf[S][u], wf[S][t], acc[u][t]) : MFMA16(wf[S][t], xf[S][u], acc[u][t]); } while (0)
#define PJ_SCHED() do { _Pragma("unroll") for (int z = 0; z < 7; ++z) { \
    __builtin_amdgcn_sched_group_barrier(0x8, 2, 0); __builtin_amdgcn_sched_group_barrier(0x100, 2, 0); } __builtin_amdgcn_sched_group_barrier(0x8, 22, 0); } while (0)
  asm volatile("s_barrier" ::: "memory");
  PJ_LOAD(0, 0, 0);
#pragma unroll 1
  for (int k = 0; k < 9; ++k) {
    PJ_LOAD(1, k, 1);
    PJ_MMA(0);
    PJ_SCHED();
    __builtin_amdgcn_sched_barrier(0);
    __builtin_amdgcn_s_waitcnt(0xC07F);
    asm volatile("s_barrier" ::: "memory");
    PJ_LOAD(0, k + 1, 0);
    PJ_MMA(1);
    PJ_SCHED();
    __builtin_amdgcn_sched_barrier(0);
  }
  PJ_LOAD(1, 9, 1);
  PJ_MMA(0);
  PJ_SCHED();
  __builtin_amdgcn_sched_barrier(0);
  PJ_MMA(1);
  const rsrc_t R = mkrsrc(ws, WS_USED);
  if (SEC != 2) {
    const int p = SEC == 3 ? 1 : 0;
#pragma unroll
    for (int u2 = 0; u2 < 2; ++u2) {
      const int tt = rb * 4 + th * 2 + u2;
      const unsigned base1 = SEC == 1 ? KV_OFF + tt * REC : Q1_OFF + (p * 32 * 64 + tt) * 1024, hs1 = SEC == 1 ? STREAM : 64 * 1024;
      const unsigned base2 = SEC == 1 ? KV_OFF + tt * REC + 1024 : Q2_OFF + (p * 32 * 64 + tt) * 256, hs2 = SEC == 1 ? STREAM : 64 * 256;
#pragma unroll
      for (int t = 0; t < 5; ++t) {
        f32x4 va = acc[2 * u2][t], vb = acc[2 * u2 + 1][t];
        if (SEC != 1) { va *= QSCALE; vb *= QSCALE; }
        const u32x2 pa = pack4(va), pb = pack4(vb);
        if (t < 4) st16(R, base1 + (cg * 8 + 4 * ch + t) * hs1 + lane * 16, pair16(pa, pb));
        else { const unsigned o2 = base2 + (cg * 8 + 4 * ch + g) * hs2 + i16 * 8; st8(R, o2, pa); st8(R, o2 + 128, pb);
               if (SEC == 1) { st8(R, o2 + 256, u32x2{0x3F803F80u, 0u}); st8(R, o2 + 384, u32x2{0x3F803F80u, 0u}); } }
      }
    }
  } else {
#pragma unroll
    for (int t = 0; t < 5; ++t) {
      const int col = cg * 160 + 80 * ch + 16 * t + i16, head = col / 20, d = col - head * 20, s_ = g & 1;
#pragma unroll
      for (int u2 = 0; u2 < 2; ++u2)
        st16(R, KV_OFF + head * STREAM + (rb * 4 + th * 2 + u2 + s_) * REC + (s_ == 0 ? OFF_VS0 : OFF_VS1) + (g >> 1) * 352 + d * 16, pair16(pack4(acc[2 * u2][t]), pack4(acc[2 * u2 + 1][t])));
    }
  }
}


template <int SEC>
__device__ __forceinline__ void proj_body(const char* x, const char* w, int ldw2, const char* wk, const char* wv, char* ws, int rb, int cg, char* lds) {
  const int lane = threadIdx.x & 63;
  const int wid = __builtin_amdgcn_readfirstlane(threadIdx.x >> 6);
  if (wid >= 4) {
    if (SEC != 3) proj_producer<SEC, 0, 0>(x, w, ldw2, wk, wv, ws, rb, cg, lds, wid - 4, lane);
    else if (cg < 2) { if (wid < 7) proj_producer<SEC, 1, 1>(x, w, ldw2, wk, wv, ws, rb, cg, lds, wid - 4, lane); else proj_producer<SEC, 0, 1>(x, w, ldw2, wk, wv, ws, rb, cg, lds, 3, lane); }
    else { if (wid < 7) proj_producer<SEC, 1, 2>(x, w, ldw2, wk, wv, ws, rb, cg, lds, wid - 4, lane); else proj_producer<SEC, 0, 2>(x, w, ldw2, wk, wv, ws, rb, cg, lds, 3, lane); }
  } else proj_consumer<SEC>(ws, rb, cg, lds, wid, lane);
}

__device__ __forceinline__ float halfmax(float m) {
  auto rr = __builtin_amdgcn_permlane32_swap(__float_as_uint(m), __float_as_uint(m), false, false);
  return fmaxf(__uint_as_float(rr[0]), __uint_as_float(rr[1]));
}
__device__ __forceinline__ float max16(const f32x16& s) { float m = s[0]; for (int i = 1; i < 16; ++i) m = fmaxf(m, s[i]); return m; }
__device__ __forceinline__ unsigned negref(float m) {
  const float hi = __uint_as_float(cvtpk(m, 0.f) << 16);
  return cvtpk(-hi, hi - m);
}
__device__ __forceinline__ f32x16 qk_raw(u32x4 k1, u32x2 k2, u32x4 q, u32x2 r) {
  f32x16 s = __builtin_amdgcn_mfma_f32_32x32x16_bf16(BF8(k1), BF8(q), f32x16{}, 0, 0, 0);
  return __builtin_amdgcn_mfma_f32_32x32x8bf16_1k(__builtin_bit_cast(s16x4, k2), __builtin_bit_cast(s16x4, r), s, 0, 0, 0);
}
__device__ __forceinline__ void true_max(const char* ws, unsigned kv, int lane, u32x4 qa, u32x4 qb, u32x2 ra, u32x2 rb, float& m0, float& m1) {
  const rsrc_t R = mkrsrc(ws, WS_USED);
  float t0 = -INFINITY, t1 = -INFINITY;
#pragma unroll 1
  for (int kt = 0; kt < 64; ++kt) {
    const u32x4 k1 = ld16(R, kv + kt * REC + lane * 16); const u32x2 k2 = ld8(R, kv + kt * REC + OFF_K2 + lane * 8);
    t0 = fmaxf(t0, max16(qk_raw(k1, k2, qa, ra))); t1 = fmaxf(t1, max16(qk_raw(k1, k2, qb, rb)));
  }
  m0 = halfmax(t0); m1 = halfmax(t1);
}

__device__ __forceinline__ void attn_phase(char* ws, char* lds, const char* wo, int wid, int lane, int p, int head, int qr, int cq) {
  const int w4 = wid & 3, hf = wid >> 2;
  const int tt0 = qr * 16 + 4 * w4;
  const unsigned kvo = KV_OFF + (p == 0 ? head : 32) * STREAM;
  const char* kv = ws + kvo;
  const unsigned lds0 = (unsigned)(uintptr_t)lds;
  const unsigned l16 = lane * 16;
  const rsrc_t R = mkrsrc(ws, WS_USED);
  const unsigned qb_ = (p * 32 + head) * 64 + tt0;
  const int hh = lane >> 5, dd = lane & 31;
  const unsigned vrel = hh * 352 + (dd <= 20 ? dd : 21) * 16;
  const unsigned gk2 = OFF_K2 + lane * 8, gv = OFF_VS0 + vrel;
  const u32x4 k1f = ld16(R, kvo + l16); const u32x2 k2f = ld8(R, kvo + gk2);
  const u32x4 q0 = ld16(R, Q1_OFF + qb_ * 1024 + l16), q1 = ld16(R, Q1_OFF + (qb_ + 1) * 1024 + l16), q2 = ld16(R, Q1_OFF + (qb_ + 2) * 1024 + l16), q3 = ld16(R, Q1_OFF + (qb_ + 3) * 1024 + l16);
  u32x2 r0 = ld8(R, Q2_OFF + qb_ * 256 + dd * 8), r1 = ld8(R, Q2_OFF + (qb_ + 1) * 256 + dd * 8), r2 = ld8(R, Q2_OFF + (qb_ + 2) * 256 + dd * 8), r3 = ld8(R, Q2_OFF + (qb_ + 3) * 256 + dd * 8);
  const unsigned rec0 = kvo + hf * 32 * REC;
  u32x4 k1t = ld16(R, rec0 + l16), v0t = ld16(R, rec0 + gv); u32x2 k2t = ld8(R, rec0 + gk2);
  __builtin_amdgcn_sched_barrier(0);
  const char* dma0 = kv + REC;
  unsigned dof[3];
  const int sp = wid < 7 ? 3 * wid : 20;
#pragma unroll
  for (int i = 0; i < 3; ++i) { const unsigned pos = (sp + i) * 1024 + l16; dof[i] = (pos >= 4 * REC ? pos - 4 * REC + 32 * REC : pos) - 1024 * i; }
  const unsigned m0a = __builtin_amdgcn_readfirstlane(lds0 + sp * 1024), m0b = m0a + SLOT;
  auto chunk0 = [&]() {
    unsigned sv;
    asm volatile("s_mov_b32 %0, m0\n\ts_mov_b32 m0, %5\n\ts_nop 0\n\t"
                 "global_load_lds_dwordx4 %1, %4 sc1\n\tglobal_load_lds_dwordx4 %2, %4 offset:1024 sc1\n\tglobal_load_lds_dwordx4 %3, %4 offset:2048 sc1\n\t"
                 "s_mov_b32 m0, %0" : "=&s"(sv) : "v"(dof[0]), "v"(dof[1]), "v"(dof[2]), "s"(uni(dma0)), "s"(m0a) : "memory");
  };
  chunk0();
  const unsigned lb = lds0 + hf * 4 * REC;
  const unsigned ak1 = lb + l16, ak2 = lb + lane * 8, av = lb + vrel;
  const unsigned idx = 0xDDDDDDDDu;
  if (hh == 1) { r0 = u32x2{0u, 0u}; r1 = u32x2{0u, 0u}; r2 = u32x2{0u, 0u}; r3 = u32x2{0u, 0u}; }
  float m0 = halfmax(max16(qk_raw(k1f, k2f, q0, r0))), m1 = halfmax(max16(qk_raw(k1f, k2f, q1, r1))), m2 = halfmax(max16(qk_raw(k1f, k2f, q2, r2))), m3 = halfmax(max16(qk_raw(k1f, k2f, q3, r3)));
  const unsigned wv = (lane / 10) * 1280 + (lane % 10) * 16;
  const char* wsrc = wo + cq * 160 + wid * 5 * 1280;
  const unsigned wdst = __builtin_amdgcn_readfirstlane(lds0 + WOIMG + wid * 800);
  f32x16 o0, o1, o2, o3;
#pragma unroll 1
  for (int pass = 0;; ++pass) {
    if (hh == 1) { r0[0] = negref(m0); r1[0] = negref(m1); r2[0] = negref(m2); r3[0] = negref(m3); }
    o0 = f32x16{}; o1 = f32x16{}; o2 = f32x16{}; o3 = f32x16{};
    unsigned cnt = 4, sv;
    const char* dma1 = dma0 + 4 * REC;
    const u32x2 k1a = {k1t[0], k1t[1]}, k1b = {k1t[2], k1t[3]}, v0a = {v0t[0], v0t[1]}, v0b = {v0t[2], v0t[3]};
    if (hf == 0) {
    asm volatile(ATTN_ASM
                 : [o0] "+v"(o0), [o1] "+v"(o1), [o2] "+v"(o2), [o3] "+v"(o3), [cnt] "+s"(cnt), [sv] "=&s"(sv)
                 : [q0] "v"(q0), [q1] "v"(q1), [q2] "v"(q2), [q3] "v"(q3), [r0] "v"(r0), [r1] "v"(r1), [r2] "v"(r2), [r3] "v"(r3),
                   [idx] "v"(idx), [k1a] "v"(k1a), [k1b] "v"(k1b), [k2] "v"(k2t), [v0a] "v"(v0a), [v0b] "v"(v0b), [d0] "v"(dof[0]), [d1] "v"(dof[1]), [d2] "v"(dof[2]),
                   [ak1] "v"(ak1), [ak2] "v"(ak2), [av] "v"(av), [wv] "v"(wv), [dma] "s"(uni(dma1)), [m0a] "s"(m0a), [m0b] "s"(m0b),
                   [wsrc] "s"(uni(wsrc)), [wdst] "s"(wdst)
                 : ATTN_CLOB);
    } else {
    asm volatile(ATTN_ASM_Y
                 : [o0] "+v"(o0), [o1] "+v"(o1), [o2] "+v"(o2), [o3] "+v"(o3), [cnt] "+s"(cnt), [sv] "=&s"(sv)
                 : [q0] "v"(q0), [q1] "v"(q1), [q2] "v"(q2), [q3] "v"(q3), [r0] "v"(r0), [r1] "v"(r1), [r2] "v"(r2), [r3] "v"(r3),
                   [idx] "v"(idx), [k1a] "v"(k1a), [k1b] "v"(k1b), [k2] "v"(k2t), [v0a] "v"(v0a), [v0b] "v"(v0b), [d0] "v"(dof[0]), [d1] "v"(dof[1]), [d2] "v"(dof[2]),
                   [ak1] "v"(ak1), [ak2] "v"(ak2), [av] "v"(av), [wv] "v"(wv), [dma] "s"(uni(dma1)), [m0a] "s"(m0a), [m0b] "s"(m0b),
                   [wsrc] "s"(uni(wsrc)), [wdst] "s"(wdst)
                 : ATTN_CLOB);
    }
    bool bad = false;
    { float sum = 0.f;
#pragma unroll
      for (int i = 0; i < 12; ++i) sum += fabsf(o0[i]) + fabsf(o1[i]) + fabsf(o2[i]) + fabsf(o3[i]);
      if (hh == 1) bad = !(o0[8] < 1.0e30f) || !(o1[8] < 1.0e30f) || !(o2[8] < 1.0e30f) || !(o3[8] < 1.0e30f);
      bad = bad || !(sum < 1.0e38f); }
    bar();
    { const f32x16& sa = hf ? o0 : o2; const f32x16& sb = hf ? o1 : o3;
      float* xw = (float*)(lds + wid * 6144) + lane * 4;
#pragma unroll
      for (int g = 0; g < 3; ++g) {
        *(f32x4*)(xw + g * 256) = f32x4{sa[4 * g], sa[4 * g + 1], sa[4 * g + 2], sa[4 * g + 3]};
        *(f32x4*)(xw + (3 + g) * 256) = f32x4{sb[4 * g], sb[4 * g + 1], sb[4 * g + 2], sb[4 * g + 3]};
      }
      const unsigned anyb = __any(bad && pass == 0);
      if (lane == 0) *(lds_vu)((lds_ptr)lds + BADW + wid * 4) = anyb; }
    bar();
    const unsigned anyb = *(lds_vu)((lds_ptr)lds + BADW + (lane & 7) * 4);
    if (!__builtin_amdgcn_readfirstlane(__any(anyb != 0))) break;
    bar();
    if (hh == 1) { r0[0] = 0; r1[0] = 0; r2[0] = 0; r3[0] = 0; }
    true_max(ws, kvo, lane, q0, q1, r0, r1, m0, m1);
    true_max(ws, kvo, lane, q2, q3, r2, r3, m2, m3);
    k1t = ld16(R, rec0 + l16); v0t = ld16(R, rec0 + gv); k2t = ld8(R, rec0 + gk2);
    chunk0();
  }
  f32x16 ka = hf ? o2 : o0, kb = hf ? o3 : o1;
  { const float* xr = (const float*)(lds + (wid ^ 4) * 6144) + lane * 4;
#pragma unroll
    for (int g = 0; g < 3; ++g) {
      f32x4 a = *(const f32x4*)(xr + g * 256), c = *(const f32x4*)(xr + (3 + g) * 256);
#pragma unroll
      for (int i = 0; i < 4; ++i) { ka[4 * g + i] += a[i]; kb[4 * g + i] += c[i]; }
    }
  }
#pragma unroll
  for (int q = 0; q < 2; ++q) {
    const f32x16& o = q ? kb : ka;
    auto rr = __builtin_amdgcn_permlane32_swap(__float_as_uint(o[8]), __float_as_uint(o[8]), false, false);
    const float inv = 1.0f / __uint_as_float(rr[1]);
    const int token = (tt0 + 2 * hf + q) * 32 + dd, u = token >> 4, t16 = token & 15;
    const u32x4 w = {cvtpk(o[0] * inv, o[1] * inv), cvtpk(o[2] * inv, o[3] * inv), cvtpk(o[4] * inv, o[5] * inv), cvtpk(o[6] * inv, o[7] * inv)};
    st16(R, O_OFF + ((p * 20 + (head >> 1)) * 128 + u) * 1024 + (2 * (head & 1) + hh) * 256 + t16 * 16, w);
    if (hh == 0) {
      const int c = head & 7;
      st8(R, O_OFF + ((p * 20 + 16 + (head >> 3)) * 128 + u) * 1024 + (c >> 1) * 256 + t16 * 16 + (c & 1) * 8, u32x2{cvtpk(o[8] * inv, o[9] * inv), cvtpk(o[10] * inv, o[11] * inv)});
    }
  }
}

template <int KH>
__device__ __forceinline__ void outproj_half(char* ws, char* out, char* lds, int p, int rb, int cq, int th, int wid, int lane, unsigned tag) {
  const int g = lane >> 4, i16 = lane & 15, q = i16 >> 2, pp = i16 & 3;
  lds_cptr wl = (lds_cptr)lds + WOIMG;
  const unsigned a1 = (20 * (g >> 1) + 4 * (g & 1) + q) * 160 + 8 * pp;
  const unsigned a2 = (40 * g + 16 + q) * 160 + 8 * pp;
#define WFRAG(j, t) ((j) < 16 ? trfrag(wl + a1 + (j) * 6400 + (t) * 32, wl + a1 + (j) * 6400 + (t) * 32 + 8 * 160) : trfrag(wl + a2 + ((j) - 16) * 25600 + (t) * 32, wl + a2 + ((j) - 16) * 25600 + (t) * 32 + 20 * 160))
  const char* fp = uni(ws + FL_OFF + 4 * (F_O + ((p * 4 + (rb >> 2)) * 8 + (rb & 3) + 4 * (th >> 1)) * 32));
  const char* ob = uni(ws + O_OFF + ((p * 20 + 10 * KH) * 128 + rb * 8 + th * 2) * 1024);
  const unsigned l16 = lane * 16, l4 = (lane & 31) * 4;
  const unsigned slot = __builtin_amdgcn_readfirstlane((unsigned)(uintptr_t)lds + POLLW + wid * 256);
  *(lds_vu)((lds_ptr)lds + POLLW + wid * 256 + lane * 4) = ~tag;
  constexpr int NPRE = 6;
  bf16x8 wp[NPRE][5];
#pragma unroll
  for (int ks = 0; ks < NPRE; ++ks)
#pragma unroll
    for (int t = 0; t < 5; ++t) wp[ks][t] = WFRAG(10 * KH + ks, t);
  u32x4 of[10][2];
#pragma unroll
  for (int ks = 0; ks < 10; ++ks) { of[ks][0] = u32x4{}; of[ks][1] = u32x4{}; }
  asm volatile("s_waitcnt lgkmcnt(0)" ::: "memory");
  unsigned issued = 0;
#pragma unroll 1
  for (unsigned it = 0; issued != 0x3FFu; ++it) {
    { unsigned sv;
      asm volatile("s_mov_b32 %0, m0\n\ts_mov_b32 m0, %3\n\ts_nop 0\n\tglobal_load_lds_dword %1, %2 sc1\n\ts_mov_b32 m0, %0" : "=&s"(sv) : "v"(l4), "s"(fp), "s"(slot) : "memory"); }
    __builtin_amdgcn_s_sleep(4);
    const unsigned v = *(lds_vu)((lds_ptr)lds + POLLW + wid * 256 + l4);
    const unsigned ready = it < SPIN_MAX ? (unsigned)__ballot(v == tag) : 0xFFFFFFFFu;
#pragma unroll
    for (int ks = 0; ks < 10; ++ks) {
      const int j = 10 * KH + ks;
      const unsigned need = j < 16 ? 3u << (2 * j) : 0xFFu << (8 * (j - 16));
      if (!((issued >> ks) & 1) && (ready & need) == need) {
        asm volatile("global_load_dwordx4 %0, %2, %3 sc1\n\tglobal_load_dwordx4 %1, %2, %3 offset:1024 sc1" : "+v"(of[ks][0]), "+v"(of[ks][1]) : "v"(l16), "s"(ob + (size_t)ks * 131072) : "memory");
        issued |= 1u << ks;
      }
    }
  }
  asm volatile("s_waitcnt vmcnt(0)"
               : "+v"(of[0][0]), "+v"(of[0][1]), "+v"(of[1][0]), "+v"(of[1][1]), "+v"(of[2][0]), "+v"(of[2][1]), "+v"(of[3][0]), "+v"(of[3][1]), "+v"(of[4][0]), "+v"(of[4][1]),
                 "+v"(of[5][0]), "+v"(of[5][1]), "+v"(of[6][0]), "+v"(of[6][1]), "+v"(of[7][0]), "+v"(of[7][1]), "+v"(of[8][0]), "+v"(of[8][1]), "+v"(of[9][0]), "+v"(of[9][1]) :: "memory");
  __builtin_amdgcn_sched_barrier(0);
  f32x4 acc[2][5];
#pragma unroll
  for (int u = 0; u < 2; ++u)
#pragma unroll
    for (int t = 0; t < 5; ++t) acc[u][t] = f32x4{};
#pragma unroll
  for (int ks = 0; ks < 10; ++ks)
#pragma unroll
    for (int t = 0; t < 5; ++t) {
      const bf16x8 wf = ks < NPRE ? wp[ks < NPRE ? ks : 0][t] : WFRAG(10 * KH + ks, t);
#pragma unroll
      for (int u = 0; u < 2; ++u) acc[u][t] = MFMA16(wf, BF8(of[ks][u]), acc[u][t]);
    }
  float* xw = (float*)(lds + wid * 5120) + lane * 4;
#pragma unroll
  for (int t = 0; t < 5; ++t) *(f32x4*)(xw + t * 256) = acc[1 - KH][t];
  bar();
  const float* xr = (const float*)(lds + (wid ^ 4) * 5120) + lane * 4;
  const rsrc_t RO = mkrsrc(out, 2048u * 2560u);
  const unsigned ob0 = (rb * 128 + th * 32 + KH * 16 + i16) * 2560 + p * 1280 + cq * 160;
  f32x4 v[5];
#pragma unroll
  for (int t = 0; t < 5; ++t) v[t] = acc[KH][t] + *(const f32x4*)(xr + t * 256);
#pragma unroll
  for (int t = 0; t < 4; t += 2) __builtin_amdgcn_raw_buffer_store_b128(pair16(pack4(v[t]), pack4(v[t + 1])), RO, ob0 + (t + (g & 1)) * 32 + (g >> 1) * 16, 0, 0);
  __builtin_amdgcn_raw_buffer_store_b64(pack4(v[4]), RO, ob0 + 128 + g * 8, 0, 0);
}

#define CODE_ADDR(sym, out) asm volatile("s_getpc_b64 s[40:41]\n\ts_add_u32 s40, s40, " sym "@rel32@lo+4\n\ts_addc_u32 s41, s41, " sym "@rel32@hi+12\n\ts_mov_b64 %0, s[40:41]" : "=s"(out) :: "s40", "s41", "scc")
__global__ __launch_bounds__(512, 2) void fused_kernel(const char* xa, const char* wqkv, const char* woa, const char* xb, const char* wq, const char* wk, const char* wv,
                                                       const char* wob, char* out, char* ws) {
  extern __shared__ __attribute__((aligned(16))) char lds[];
  const int lane = threadIdx.x & 63;
  const int wid = __builtin_amdgcn_readfirstlane(threadIdx.x >> 6);
  const int b = blockIdx.x, x = b & 7, j = b >> 3;
  const unsigned hsh = (unsigned)amdgcn_dispatch_id() * 2654435761u + 0x7F4A7C15u;
  const unsigned tag = __builtin_amdgcn_readfirstlane((hsh & 0x00FFFFFFu) | ((~hsh & 0xFFu) << 24));
  unsigned cpf = 0;
  if (threadIdx.x < 24) {
    unsigned long long pa;
    CODE_ADDR(FUSED_SYM, pa);
    const unsigned off = (j * 24 + threadIdx.x) * 128;
    if (off < CODE_BYTES) cpf = *(const volatile unsigned*)(pa + off);
  }
  if (threadIdx.x >= 64 && threadIdx.x < 64 + 13) {
    const unsigned i = j * 13 + (threadIdx.x - 64), st = TLBPF_STRIDE;
    const char* a = nullptr;
    if (i < 288) a = ws + (size_t)i * st; else if (i < 368) a = out + (size_t)(i - 288) * st; else if (i < 381) a = woa + (size_t)(i - 368) * st; else if (i < 394) a = wob + (size_t)(i - 381) * st;
    if (a) cpf = *(const volatile unsigned*)a;
  }
  { const int rb = (x & 1) * 8 + (j & 7), cg = j >> 3, sec = x >> 1;
    if (sec == 0) proj_body<0>(xa, wqkv + cg * 320, 3840, nullptr, nullptr, ws, rb, cg, lds);
    else if (sec == 1) proj_body<1>(xa, wqkv + 1280 + cg * 320, 3840, nullptr, nullptr, ws, rb, cg, lds);
    else if (sec == 2) proj_body<2>(xa, wqkv + 2560 + cg * 320, 3840, nullptr, nullptr, ws, rb, cg, lds);
    else proj_body<3>(xb, wq + cg * 320, 1280, wk, wv, ws, rb, cg, lds);
    asm volatile("" :: "v"(cpf));
    waitbar<0>();
    if (wid == 0) {
      if (sec < 3) { if (lane < 4) flag_set(ws, F_PA + (cg * 4 + lane) * 64 + sec * 16 + rb, tag); }
      else if (lane < 8) flag_set(ws, F_PB + lane * 64 + cg * 16 + rb, tag);
    } }
  const int p = j >> 4, head = x * 4 + ((j & 15) >> 2), qr = j & 3;
  const int po = x >> 2, rbo = (x & 3) * 4 + (j & 3), cq = j >> 2;
  if (wid == 0) {
    int idx;
    if (p == 0) idx = lane < 4 ? F_PA + ((head >> 3) * 4 + qr) * 64 + 4 * qr + lane : (lane >= 16 && lane < 48 ? F_PA + ((head >> 3) * 4 + qr) * 64 + lane : -1);
    else idx = F_PB + x * 64 + lane;
    flags_wait(ws, idx, tag);
  }
  bar();
  attn_phase(ws, lds, po ? wob : woa, wid, lane, p, head, qr, cq);
  waitvm<0>();
  if (lane == 0) flag_set(ws, F_O + ((p * 4 + qr) * 8 + wid) * 32 + head, tag);
  bar();
  if (wid < 4) outproj_half<0>(ws, out, lds, po, rbo, cq, wid & 3, wid, lane, tag); else outproj_half<1>(ws, out, lds, po, rbo, cq, wid & 3, wid, lane, tag);
}

extern "C" void kernel_launch(void* const* d_in, const int* in_sizes, int n_in, void* d_out, int out_size, void* d_ws, size_t ws_size, hipStream_t stream) {
  (void)hipFuncSetAttribute((const void*)fused_kernel, hipFuncAttributeMaxDynamicSharedMemorySize, LDS_BYTES);
  fused_kernel<<<256, 512, LDS_BYTES, stream>>>((const char*)d_in[0], (const char*)d_in[1], (const char*)d_in[2], (const char*)d_in[3], (const char*)d_in[4],
                                                (const char*)d_in[5], (const char*)d_in[6], (const char*)d_in[7], (char*)d_out, (char*)d_ws);
}
```
